```python
import torch

def _softmax(x, axis=-1):
    return torch.softmax(x, dim=axis)
def _gelu(x):
    return torch.nn.functional.gelu(x, approximate="tanh")
def _silu(x):
    return torch.nn.functional.silu(x)
def _sigmoid(x):
    return torch.sigmoid(x)
def _layernorm(x, axis=-1, eps=1e-5):
    m = x.mean(dim=axis, keepdim=True)
    v = x.var(dim=axis, keepdim=True, unbiased=False)
    return (x - m) / torch.sqrt(v + eps)
def _scatter_add(tgt, idx, src, axis=0):
    return tgt.clone().index_add_(axis, idx, src)
def _segment_sum(src, seg, n):
    out = torch.zeros((n,) + src.shape[1:], dtype=src.dtype)
    return out.index_add_(0, seg, src)
def _pad(x, pw):
    flat = tuple(v for pair in reversed(pw) for v in pair)
    return torch.nn.functional.pad(x, flat)

kernel_name = 'parallel_sum__linear_attn_block__mixer_channel_mlp__prompt'

def setup_inputs():

    _w = (lambda a: (lambda t: t.to(torch.bfloat16) if t.is_floating_point() else t)(torch.as_tensor(a)))
    def _A_setup():
        B, S, D = 1, 2048, 448
        return {
            "x": _w((lambda *s: torch.randn(*s, dtype=torch.bfloat16) * 0.05)(B, S, D)),
            "wqkv": _w((lambda *s: torch.randn(*s, dtype=torch.bfloat16) * 0.1)(D, 3 * D)),
            "wo": _w((lambda *s: torch.randn(*s, dtype=torch.bfloat16) * 0.05)(D, D)),
        }
    _A_in = _A_setup()

    def _B_setup():
        B, S, D, F = 1, 2048, 448, 1792
        return {
            "x": _w((lambda *s: torch.randn(*s, dtype=torch.bfloat16) * 0.05)(B, S, D)),
            "w1": _w((lambda *s: torch.randn(*s, dtype=torch.bfloat16) * 1.6)(D, F) * (D ** -0.5)),
            "b1": _w((lambda *s: torch.randn(*s, dtype=torch.bfloat16) * 0.4)(F)),
            "w2": _w((lambda *s: torch.randn(*s, dtype=torch.bfloat16) * 0.05)(F, D) * (F ** -0.5)),
            "b2": _w((lambda *s: torch.randn(*s, dtype=torch.bfloat16) * 0.05)(D)),
        }
    _B_in = _B_setup()

    return {
        "A_x": _A_in["x"],
        "A_wqkv": _A_in["wqkv"],
        "A_wo": _A_in["wo"],
        "B_x": _B_in["x"],
        "B_w1": _B_in["w1"],
        "B_b1": _B_in["b1"],
        "B_w2": _B_in["w2"],
        "B_b2": _B_in["b2"],
    }

def reference(A_x, A_wqkv, A_wo, B_x, B_w1, B_b1, B_w2, B_b2):
    def _A_ref(x, wqkv, wo):
        B, S, D = x.shape
        H = 16; Dh = D // H
        qkv = (lambda x, *d: x.permute(*d))((x @ wqkv).reshape(B, S, 3, H, Dh), 2, 0, 3, 1, 4)
        q, k, v = qkv[0], qkv[1], qkv[2]
        phi = lambda t: torch.where(t > 0, t + 1, torch.exp(t)) + 1e-6
        pq, pk = phi(q), phi(k)
        kv = torch.einsum("bhsd,bhse->bhde", pk, v)
        z = torch.einsum("bhsd->bhd", pk)
        num = torch.einsum("bhqd,bhde->bhqe", pq, kv)
        den = torch.einsum("bhqd,bhd->bhq", pq, z)[..., None] + 1e-6
        o = num / den
        return (lambda x, *d: x.permute(*d))(o, 0, 2, 1, 3).reshape(B, S, D) @ wo

    def _B_ref(x, w1, b1, w2, b2):
        y = _layernorm(x, axis=-1)
        h = _gelu(torch.einsum("bsd,df->bsf", y, w1) + b1)
        return torch.einsum("bsf,fd->bsd", h, w2) + b2

    x = A_x
    xn = _layernorm(x, axis=-1)
    return x + _A_ref(xn, A_wqkv, A_wo) + _B_ref(xn, B_w1, B_b1, B_w2, B_b2)
```

```cpp
#include <hip/hip_runtime.h>
#include <hip/hip_bf16.h>
#include <stdint.h>

typedef unsigned short u16;
static constexpr int S = 2048, D = 448, H = 16, DH = 28, F = 1792;

__device__ __forceinline__ float bf2f(u16 v) { return __uint_as_float(((unsigned)v) << 16); }
__device__ __forceinline__ u16 f2bf(float f) { unsigned u = __float_as_uint(f); u += 0x7fffu + ((u >> 16) & 1u); return (u16)(u >> 16); }

__global__ void k_ln(const u16* x, float* xn, float* y) {
    int row = blockIdx.x; int lane = threadIdx.x;
    float v[7]; float s = 0.f;
    for (int i = 0; i < 7; ++i) { v[i] = bf2f(x[row * D + lane + 64 * i]); s += v[i]; }
    for (int m = 32; m >= 1; m >>= 1) s += __shfl_xor(s, m);
    float mean = s / D; float q = 0.f;
    for (int i = 0; i < 7; ++i) { float d = v[i] - mean; q += d * d; }
    for (int m = 32; m >= 1; m >>= 1) q += __shfl_xor(q, m);
    float r = rsqrtf(q / D + 1e-5f);
    s = 0.f;
    for (int i = 0; i < 7; ++i) { v[i] = bf2f(f2bf((v[i] - mean) * r)); xn[row * D + lane + 64 * i] = v[i]; s += v[i]; }
    for (int m = 32; m >= 1; m >>= 1) s += __shfl_xor(s, m);
    mean = s / D; q = 0.f;
    for (int i = 0; i < 7; ++i) { float d = v[i] - mean; q += d * d; }
    for (int m = 32; m >= 1; m >>= 1) q += __shfl_xor(q, m);
    r = rsqrtf(q / D + 1e-5f);
    for (int i = 0; i < 7; ++i) y[row * D + lane + 64 * i] = bf2f(f2bf((v[i] - mean) * r));
}
__global__ void k_gemm(const float* A, const u16* B, const u16* bias, float* C, int M, int N, int K, int mode) {
    int n = blockIdx.x * blockDim.x + threadIdx.x; int m = blockIdx.y;
    if (n >= N) return;
    float acc = 0.f;
    for (int k = 0; k < K; ++k) acc += A[m * K + k] * bf2f(B[k * N + n]);
    if (mode == 1) { float t = acc + bf2f(bias[n]); float u = 0.7978845608028654f * (t + 0.044715f * t * t * t); acc = 0.5f * t * (1.f + tanhf(u)); }
    C[m * N + n] = acc;
}
__device__ __forceinline__ float phi(float t) { return (t > 0.f ? t + 1.f : __expf(t)) + 1e-6f; }
__global__ void k_kv(const float* qkv, float* kv) {
    int h = blockIdx.x / DH, d = blockIdx.x % DH; int e = threadIdx.x;
    if (e > DH) return;
    float acc = 0.f;
    for (int s = 0; s < S; ++s) { float pk = phi(qkv[s * 3 * D + D + h * DH + d]); float v = e < DH ? qkv[s * 3 * D + 2 * D + h * DH + e] : 1.f; acc += pk * v; }
    kv[(h * DH + d) * 32 + e] = acc;
}
__global__ void k_o(const float* qkv, const float* kv, float* o) {
    int s = blockIdx.x; int c = threadIdx.x; if (c >= D) return;
    int h = c / DH, e = c % DH;
    float num = 0.f, den = 0.f;
    for (int d = 0; d < DH; ++d) { float pq = phi(qkv[s * 3 * D + h * DH + d]); num += pq * kv[(h * DH + d) * 32 + e]; den += pq * kv[(h * DH + d) * 32 + DH]; }
    o[s * D + c] = num / (den + 1e-6f);
}
__global__ void k_fin(const u16* x, const float* a, const float* b, const u16* b2, u16* out) {
    int i = blockIdx.x * blockDim.x + threadIdx.x; if (i >= S * D) return;
    out[i] = f2bf(bf2f(x[i]) + a[i] + b[i] + bf2f(b2[i % D]));
}

extern "C" void kernel_launch(void* const* d_in, const int* in_sizes, int n_in, void* d_out, int out_size, void* d_ws, size_t ws_size, hipStream_t stream) {
    const u16* x = (const u16*)d_in[0]; const u16* wqkv = (const u16*)d_in[1]; const u16* wo = (const u16*)d_in[2];
    const u16* w1 = (const u16*)d_in[4]; const u16* b1 = (const u16*)d_in[5]; const u16* w2 = (const u16*)d_in[6]; const u16* b2 = (const u16*)d_in[7];
    float* ws = (float*)d_ws;
    float* xn = ws; float* y = xn + S * D; float* qkv = y + S * D; float* h = qkv + S * 3 * D; float* kv = h + S * F; float* o = kv + H * DH * 32; float* a = o + S * D; float* b = a + S * D;
    k_ln<<<S, 64, 0, stream>>>(x, xn, y);
    k_gemm<<<dim3((3 * D + 255) / 256, S), 256, 0, stream>>>(xn, wqkv, nullptr, qkv, S, 3 * D, D, 0);
    k_gemm<<<dim3((F + 255) / 256, S), 256, 0, stream>>>(y, w1, b1, h, S, F, D, 1);
    k_kv<<<H * DH, 64, 0, stream>>>(qkv, kv);
    k_o<<<S, 448, 0, stream>>>(qkv, kv, o);
    k_gemm<<<dim3((D + 255) / 256, S), 256, 0, stream>>>(o, wo, nullptr, a, S, D, D, 0);
    k_gemm<<<dim3((D + 255) / 256, S), 256, 0, stream>>>(h, w2, nullptr, b, S, D, F, 0);
    k_fin<<<(S * D + 255) / 256, 256, 0, stream>>>(x, a, b, b2, (u16*)d_out);
}
```

```cpp
#pragma clang diagnostic ignored "-Wint-to-pointer-cast"
#include <hip/hip_runtime.h>
#include <stdint.h>

typedef unsigned short u16;
typedef unsigned u32;
typedef unsigned long long u64;
using bf16x8 = __attribute__((ext_vector_type(8))) short;
using s16x4 = __attribute__((ext_vector_type(4))) short;
using f32x16 = __attribute__((ext_vector_type(16))) float;
using f32x4 = __attribute__((ext_vector_type(4))) float;
using f32x2 = __attribute__((ext_vector_type(2))) float;
using u32x4 = __attribute__((ext_vector_type(4))) unsigned;
using u32x2 = __attribute__((ext_vector_type(2))) unsigned;
#define LAS __attribute__((address_space(3)))
#define GAS __attribute__((address_space(1)))
#define DEV __device__ __forceinline__

#define BSL 1
#define H1I 30
#define H1C 36
#define H2I 48
#define H2C 54
#define FL01 74

namespace {
constexpr int DM = 448, FF = 1792;
constexpr int NSLAB = 81, RING = 12, SLABB = 8192;
constexpr int L_XN = RING * SLABB;
constexpr int XS = 912, HS = 592;
constexpr int L_H = L_XN;
constexpr int L_KVX = L_XN + 64 * HS;
constexpr int L_RED = L_KVX + 16384;
constexpr int L_MISC = L_XN + 64 * XS;
constexpr int L_R2 = L_MISC;
constexpr int L_B1 = L_MISC + 256;
constexpr int L_KVB = L_B1 + 1024;
constexpr int L_DUMMY = L_KVB + 5120;
constexpr int LDS_BYTES = L_DUMMY + 256;
static_assert(L_RED + 2048 <= L_MISC, "lds");
static_assert(LDS_BYTES <= 163840, "lds");
constexpr size_t W_E = 0, W_FL = 4096, W_P = 0x20000, W_R = W_P + (4u << 20), W_O = W_R + 65536;
constexpr size_t W_TS = 32u << 20;
constexpr u32 SPIN_CAP = 1u << 18;

typedef __bf16 bf16x2_t __attribute__((ext_vector_type(2)));
DEV u32 cvtpk(float lo, float hi) { f32x2 v = {lo, hi}; return __builtin_bit_cast(u32, __builtin_convertvector(v, bf16x2_t)); }
DEV float bflo(u32 v) { return __uint_as_float(v << 16); }
DEV float bfhi(u32 v) { return __uint_as_float(v & 0xffff0000u); }
template <int CTRL> DEV float dpp(float v) { return __int_as_float(__builtin_amdgcn_update_dpp(0, __float_as_int(v), CTRL, 0xf, 0xf, true)); }
template <int IMM> DEV void glds16(const char* sb, u32 voff, u32 ldsw) {
    asm volatile("s_add_u32 m0, %2, %3\n\ts_nop 0\n\tglobal_load_lds_dwordx4 %0, %1" :: "v"(voff), "s"(sb), "s"(ldsw), "n"(IMM) : "memory", "scc");
}
template <int IMM> DEV void glds16c(const char* sb, u32 voff, u32 ldsw) {
    asm volatile("s_add_u32 m0, %2, %3\n\ts_nop 0\n\tglobal_load_lds_dwordx4 %0, %1 sc1" :: "v"(voff), "s"(sb), "s"(ldsw), "n"(IMM) : "memory", "scc");
}
DEV void touch(const char* sb, u32 voff, u32 lds) {
    asm volatile("s_mov_b32 m0, %2\n\ts_nop 0\n\tglobal_load_lds_dword %0, %1" :: "v"(voff), "s"(sb), "s"(lds) : "memory");
}
DEV void st16c(char* sb, u32 voff, u32x4 v) { asm volatile("global_store_dwordx4 %0, %1, %2 sc1\n\ts_nop 1" :: "v"(voff), "v"(v), "s"(sb) : "memory"); }
DEV void st8c(char* sb, u32 voff, u32x2 v) { asm volatile("global_store_dwordx2 %0, %1, %2 sc1" :: "v"(voff), "v"(v), "s"(sb) : "memory"); }
template <int N> DEV void waitvm() { asm volatile("s_waitcnt vmcnt(%0)" :: "n"(N) : "memory"); }
template <typename T> DEV T lds_ld(u32 a) { return *(const LAS T*)(a); }
template <typename T> DEV void lds_st(u32 a, T v) { *(LAS T*)(a) = v; }
typedef short v4i16_t __attribute__((ext_vector_type(4)));
DEV bf16x8 trfrag(u32 a) {
    s16x4 lo = __builtin_amdgcn_ds_read_tr16_b64_v4i16((LAS v4i16_t*)(a));
    s16x4 hi = __builtin_amdgcn_ds_read_tr16_b64_v4i16((LAS v4i16_t*)(a + 1024));
    return __builtin_shufflevector(lo, hi, 0, 1, 2, 3, 4, 5, 6, 7);
}
#define MFMA32(a, b, c) __builtin_amdgcn_mfma_f32_32x32x16_bf16(a, b, c, 0, 0, 0)
#define MFMA16(a, b, c) __builtin_amdgcn_mfma_f32_16x16x32_bf16(a, b, c, 0, 0, 0)
#define SBAR() __builtin_amdgcn_sched_barrier(0)

DEV float phi(float t) { float e = __builtin_amdgcn_exp2f(t * 1.4426950408889634f); return (t > 0.f ? t + 1.f : e) + 1e-6f; }
DEV float gelu(float u) {
    const float k0 = -2.f * 0.7978845608028654f * 1.4426950408889634f, k1 = k0 * 0.044715f;
    float p = __builtin_fmaf(u * u, k1, k0);
    float e = __builtin_amdgcn_exp2f(u * p);
    return u * __builtin_amdgcn_rcpf(1.f + e);
}

constexpr int nglds(int s) { return s < 2 ? 0 : ((s + 10 < NSLAB) + (s + 11 < NSLAB)); }
constexpr int extra(int s) { return (s == 16 ? 2 : 0) + (s == H1I ? 2 : 0) + (s == H2I ? 1 : 0) + (s == 64 ? 2 : 0) + (s == 66 ? 2 : 0) + (s == FL01 ? 2 : 0); }
constexpr int younger_than_extra(int from, int upto) { int n = 0; for (int i = from + 2; i < upto; i += 2) n += nglds(i) + extra(i); return n; }
constexpr int vmwait(int s) {
    if (s + 3 <= 11) { int n = 11 - (s + 3); for (int i = 0; i < s; i += 2) n += nglds(i) + extra(i); return n; }
    int last = (s + 3 < NSLAB) ? s - 8 : 70;
    return extra(last) + younger_than_extra(last, s);
}

struct Ctx {
    const char *gKV, *gW1a, *gW1b, *gQ, *gW2, *gWo;
    u32 vKV, vQ, vW1, vW2, vWo1, vL, vLd, vLo;
    u32 ldsw;
};
template <int SG> DEV void issue(const Ctx& c) {
    constexpr int IMM = (SG % RING) * SLABB;
    if constexpr (SG < 14) glds16<IMM>(c.gKV + SG * 32 * 2688, c.vKV, c.ldsw);
    else if constexpr (SG < 28) glds16<IMM>(c.gW1a + (SG - 14) * 32 * 3584, c.vW1, c.ldsw);
    else if constexpr (SG < 42) glds16<IMM>(c.gW1b + (SG - 28) * 32 * 3584, c.vW1, c.ldsw);
    else if constexpr (SG < 49) glds16<IMM>(c.gQ + (SG - 42) * 32 * 2688, c.vQ, c.ldsw);
    else if constexpr (SG < 56) glds16<IMM>(c.gW2 + (SG - 49) * 32 * 896, c.vW2, c.ldsw);
    else if constexpr (SG < 63) glds16<IMM>(c.gW2 + (SG - 56) * 32 * 896 + 256, c.vW2, c.ldsw);
    else if constexpr (SG == 63) glds16<IMM>(c.gWo, c.vW2, c.ldsw);
    else if constexpr (SG == 64) glds16<IMM>(c.gWo, c.vWo1, c.ldsw);
    else if constexpr (SG == 65) glds16<IMM>(c.gWo + 256, c.vW2, c.ldsw);
    else if constexpr (SG == 66) glds16<IMM>(c.gWo + 256, c.vWo1, c.ldsw);
    else if constexpr (SG < 74) glds16<IMM>(c.gW2 + (SG - 67) * 32 * 896 + 512, c.vW2, c.ldsw);
    else if constexpr (SG == 74) glds16<IMM>(c.gWo + 512, c.vW2, c.ldsw);
    else if constexpr (SG == 75) glds16<IMM>(c.gWo + 512, c.vWo1, c.ldsw);
    else if constexpr (SG < 79) glds16<IMM>(c.gW2 + (SG - 76) * 64 * 896 + 768, c.vL, c.ldsw);
    else if constexpr (SG == 79) glds16<IMM>(c.gW2 + 192 * 896 + 768, c.vLd, c.ldsw);
    else glds16<IMM>(c.gWo + 768, c.vLo, c.ldsw);
}
template <int SG, int VM, int LG> DEV void sync_raw(const Ctx& c) {
    SBAR();
    asm volatile("s_waitcnt vmcnt(%0) lgkmcnt(%1)\n\ts_barrier" :: "n"(VM), "n"(LG) : "memory");
    if constexpr (SG >= 2 && SG + 10 < NSLAB) issue<SG + 10>(c);
    if constexpr (SG >= 2 && SG + 11 < NSLAB) issue<SG + 11>(c);
    SBAR();
}
constexpr u32 slot_off(int sg) { return (u32)((sg % RING) * SLABB); }
}

#define STAMPR(i)

__global__ __launch_bounds__(512, 2) void fused_block(const u16* __restrict__ x, const u16* __restrict__ wqkv, const u16* __restrict__ wo,
                                                      const u16* __restrict__ w1, const u16* __restrict__ b1, const u16* __restrict__ w2,
                                                      const u16* __restrict__ b2, u16* __restrict__ out, char* __restrict__ ws) {
    constexpr u32 L0 = 0;
    asm volatile("" :: "s"(x), "s"(wqkv), "s"(wo), "s"(w1), "s"(b1), "s"(w2), "s"(b2), "s"(out), "s"(ws));
    const int lane = threadIdx.x & 63;
    const int w = __builtin_amdgcn_readfirstlane(threadIdx.x >> 6);
    const int th = w & 1, g = w >> 1;
    const int xcd = blockIdx.x & 7, jj = blockIdx.x >> 3;
    const int s = (xcd % (8 / BSL)) * BSL + (jj % BSL);
    const int t = (xcd / (8 / BSL)) * (32 / BSL) + jj / BSL;
    STAMPR(0);

    u32x4 xr[7];
    const int xrow = 8 * w + (lane >> 3);
    const int rot = (jj % BSL) * (8 / BSL);
    {
        const char* xb = (const char*)x + (size_t)t * (64 * 896);
        const u32 vx = xrow * 896 + (lane & 7) * 16;
#pragma unroll
        for (int j = 0; j < 7; ++j) {
            int jr = j + rot; jr = jr >= 7 ? jr - 7 : jr;
            asm volatile("global_load_dwordx4 %0, %1, %2" : "=v"(xr[j]) : "v"(vx), "s"(xb + jr * 128));
        }
    }
    Ctx c;
    {
        const int r = lane >> 4, ch = (lane & 15) ^ (r << 2), seg = ch >> 3, c8 = ch & 7, row = 4 * w + r;
        const int c7 = c8 > 6 ? 6 : c8;
        c.vKV = row * 2688 + seg * 896 + c7 * 16;
        c.vQ = (row + seg * 224) * 2688 + c7 * 16;
        c.vW1 = row * 3584 + ch * 16;
        c.vW2 = row * 896 + ch * 16;
        c.vWo1 = (row + 32 > 55 ? 55 : row + 32) * 896 + ch * 16;
        c.vL = (row + 32 * seg) * 896 + c8 * 16;
        c.vLd = row * 896 + c8 * 16;
        c.vLo = (row + 32 * seg > 55 ? 55 : row + 32 * seg) * 896 + c8 * 16;
        c.gKV = (const char*)wqkv + 896 + 112 * s;
        c.gQ = (const char*)wqkv + 112 * s;
        const int wb = 448 * s - 64 * (s & 1);
        c.gW1a = (const char*)w1 + wb + ((s & 1) ? 256 : 0);
        c.gW1b = (const char*)w1 + wb + ((s & 1) ? 0 : 256);
        c.gW2 = (const char*)w2 + (size_t)s * (224 * 896);
        c.gWo = (const char*)wo + (size_t)s * (56 * 896);
        c.ldsw = L0 + w * 1024;
    }
    constexpr int NPF = 2;
    {
        const int s0 = s - (jj % BSL);
        const int tid = threadIdx.x;
        {
            constexpr int LPS = (BSL == 1) ? 2 : (BSL == 2 ? 3 : 4);
            const int base = (112 * s0) & ~127;
            int i = tid; const int n = 14 * 3 * LPS;
            u32 off;
            const char* sb = (const char*)wqkv;
            i = i < n ? i : n - 1;
            const int m = i / (14 * LPS), rem = i % (14 * LPS), rr = rem / LPS, ll = rem % LPS;
            const int mm = m == 2 ? 0 : m + 1;
            int col = base + ll * 128; col = col > 768 ? 768 : col;
            off = (14 * jj + rr) * 2688 + mm * 896 + col;
            if (w * 64 < n) touch(sb, off, L0 + L_DUMMY);
            else {
                constexpr int LW = (BSL == 1) ? 4 : (7 * BSL / 2);
                constexpr int W0 = (14 * 3 * LPS + 63) / 64;
                int k = tid - W0 * 64; const int n1 = 14 * LW;
                k = k < n1 ? k : n1 - 1;
                const int bw = (448 * s0) & ~127;
                touch((const char*)w1, (14 * jj + k / LW) * 3584 + bw + (k % LW) * 128, L0 + L_DUMMY);
            }
        }
        {
            constexpr int N2 = 49 * BSL, NO = (392 * BSL + 31) / 32;
            if (w == 7) {
                const int wb = 448 * s - 64 * (s & 1);
                glds16<L_B1>((const char*)b1 + wb, (lane & 31) * 16, L0);
            } else if (w >= 5) {
                int k = jj * NO + (tid - 320 < NO ? tid - 320 : NO - 1); k = k < 392 * BSL ? k : 392 * BSL - 1;
                touch((const char*)wo + (size_t)s0 * (56 * 896), k * 128, L0 + L_DUMMY);
            } else {
                int k = tid < N2 ? tid : N2 - 1;
                touch((const char*)w2 + (size_t)s0 * (224 * 896), (jj * N2 + k) * 128, L0 + L_DUMMY);
            }
        }
    }
    issue<0>(c); issue<1>(c); issue<2>(c); issue<3>(c); issue<4>(c); issue<5>(c);
    issue<6>(c); issue<7>(c); issue<8>(c); issue<9>(c); issue<10>(c); issue<11>(c);
    u64 epoch;
    asm volatile("s_load_dwordx2 %0, %1, 0x0" : "=s"(epoch) : "s"(ws));

    asm volatile("s_waitcnt vmcnt(%8) lgkmcnt(0)" : "+v"(xr[0]), "+v"(xr[1]), "+v"(xr[2]), "+v"(xr[3]), "+v"(xr[4]), "+v"(xr[5]), "+v"(xr[6]), "+s"(epoch) : "n"(12 + NPF));
    STAMPR(1);
    const u64 tag64 = epoch + 0x100000001ull;
    const u32 tag = (u32)tag64;
    {
        float s1 = 0.f, s2 = 0.f, s1b = 0.f, s2b = 0.f;
#pragma unroll
        for (int j = 0; j < 7; ++j)
#pragma unroll
            for (int e = 0; e < 4; ++e) {
                float a = bflo(xr[j][e]), b = bfhi(xr[j][e]);
                s1 += a; s2 = __builtin_fmaf(a, a, s2); s1b += b; s2b = __builtin_fmaf(b, b, s2b);
            }
        s1 += s1b; s2 += s2b;
        s1 += dpp<0xB1>(s1); s2 += dpp<0xB1>(s2);
        s1 += dpp<0x4E>(s1); s2 += dpp<0x4E>(s2);
        s1 += dpp<0x141>(s1); s2 += dpp<0x141>(s2);
        const float mean = s1 * (1.f / DM);
        const float var = __builtin_fmaxf(__builtin_fmaf(-mean, mean, s2 * (1.f / DM)), 0.f);
        const float rstd = __builtin_amdgcn_rsqf(var + 1e-5f);
        const float nb = -mean * rstd;
        const u32 xa = L0 + L_XN + xrow * XS + (lane & 7) * 16;
#pragma unroll
        for (int j = 0; j < 7; ++j) {
            int jr = j + rot; jr = jr >= 7 ? jr - 7 : jr;
            u32x4 o;
#pragma unroll
            for (int e = 0; e < 4; ++e) o[e] = cvtpk(__builtin_fmaf(bflo(xr[j][e]), rstd, nb), __builtin_fmaf(bfhi(xr[j][e]), rstd, nb));
            lds_st<u32x4>(xa + jr * 128, o);
        }
        if ((lane & 7) == 0) lds_st<float>(L0 + L_R2 + xrow * 4, __builtin_amdgcn_rsqf(var * rstd * rstd + 1e-5f));
    }
    asm volatile("s_waitcnt lgkmcnt(0)\n\ts_barrier" ::: "memory");
    const int l31 = lane & 31, hf = lane >> 5;
    bf16x8 A[28];
    {
        const u32 a = L0 + L_XN + (32 * th + l31) * XS + hf * 16;
#pragma unroll
        for (int j = 0; j < 28; ++j) A[j] = lds_ld<bf16x8>(a + j * 32);
    }
    const float r2 = lds_ld<float>(L0 + L_R2 + (32 * th + l31) * 4);

    const int blk = (lane >> 4) & 1, q = (lane & 15) >> 2, p = lane & 3;
    const u32 trrow = L0 + (8 * hf + q) * 256;
    const u32 mstd = 32 * blk + 8 * p, mperm = 32 * (p & 1) + 16 * blk + 8 * (p >> 1);
    const u32 tKV = trrow + ((((g >> 1) * 128 + (g & 1) * 56) + mstd) ^ (q << 6));
    const int gq = (s & 1) ? 3 - g : g;
    const u32 tW1 = trrow + ((64 * gq + mperm) ^ (q << 6));
    const u32 tQ = trrow + (((g & 1) * 56 + mperm) ^ (q << 6));
    const u32 tO = trrow + ((64 * g + mperm) ^ (q << 6));
    const u32 hrow = L0 + L_H + (32 * th + l31) * HS;

    char* const wsP = ws + W_P + (size_t)s * (32 * 32 * 512);
    char* const wsR = ws + W_R + (size_t)s * 8192;
    char* const wsO = ws + W_O + (size_t)t * (8 * 7 * 8 * 1024);
    char* const wsF = ws + W_FL + (size_t)t * (8 * 256);

    bf16x8 Bc0, Bc1, Bn0, Bn1;
    f32x16 acc, accP;
    const f32x16 zero16 = {0.f, 0.f, 0.f, 0.f, 0.f, 0.f, 0.f, 0.f, 0.f, 0.f, 0.f, 0.f, 0.f, 0.f, 0.f, 0.f};

    auto hop1_issue = [&]() { const char* src = wsP + (size_t)t * 16384 + w * 2048; glds16c<L_KVX>(src, lane * 16, L0 + w * 2048); glds16c<L_KVX + 1024>(src + 1024, lane * 16, L0 + w * 2048); };
    auto hop1_check = [&]() {
        u32x4 a, b;
        for (u32 spin = 0;; ++spin) {
            a = lds_ld<u32x4>(L0 + L_KVX + w * 2048 + lane * 16); b = lds_ld<u32x4>(L0 + L_KVX + w * 2048 + 1024 + lane * 16);
            const bool ok = a[1] == tag && a[3] == tag && b[1] == tag && b[3] == tag;
            if (__all(ok) || spin >= SPIN_CAP) break;
            hop1_issue(); waitvm<0>();
        }
        float v0 = __uint_as_float(a[0]) + __uint_as_float(b[0]), v1 = __uint_as_float(a[2]) + __uint_as_float(b[2]);
        auto r0 = __builtin_amdgcn_permlane32_swap(__float_as_uint(v0), __float_as_uint(v0), false, false);
        auto r1 = __builtin_amdgcn_permlane32_swap(__float_as_uint(v1), __float_as_uint(v1), false, false);
        f32x2 o = {__uint_as_float(r0[0]) + __uint_as_float(r0[1]), __uint_as_float(r1[0]) + __uint_as_float(r1[1])};
        if (lane < 32) lds_st<f32x2>(L0 + L_RED + w * 256 + lane * 8, o);
    };
    auto hop1_publish = [&]() {
        if (w == 6) {
            f32x2 v = lds_ld<f32x2>(L0 + L_RED + l31 * 8);
#pragma unroll
            for (int i = 1; i < 8; ++i) v += lds_ld<f32x2>(L0 + L_RED + i * 256 + l31 * 8);
            if (lane < 32) st8c(wsR + t * 256, lane * 8, (u32x2){cvtpk(v[0], v[1]), tag});
        }
    };
    auto hop2_issue = [&]() { glds16c<L_KVX>(wsR + w * 1024, lane * 16, L0 + w * 2048); };
    auto hop2_check = [&]() {
        u32x4 a;
        for (u32 spin = 0;; ++spin) {
            a = lds_ld<u32x4>(L0 + L_KVX + w * 2048 + lane * 16);
            const bool ok = a[1] == tag && a[3] == tag;
            if (__all(ok) || spin >= SPIN_CAP) break;
            hop2_issue(); waitvm<0>();
        }
        const int Ln = 32 * ((lane >> 4) & 1) + 2 * (lane & 15);
        const u32 ad = L0 + L_KVB + (w >> 2) * 2560 + (16 * (w & 1) + (Ln & 15)) * 80 + (16 * ((w >> 1) & 1) + 4 * (Ln >> 4) + 2 * hf) * 2;
        lds_st<u32>(ad, a[0]); lds_st<u32>(ad + 80, a[2]);
    };
#define SYNC(SG, LG) do { \
        constexpr bool full_ = ((SG) == 16 || (SG) == H1C + 2 || (SG) == 48 || (SG) == H2C + 2 || (SG) == H2C + 4); \
        constexpr int vmr_ = vmwait(SG); \
        constexpr int vm1_ = (SG) == H1C ? younger_than_extra(H1I, SG) : ((SG) == H2C ? younger_than_extra(H2I, SG) : ((SG) == FL01 ? younger_than_extra(66, SG) : 63)); \
        sync_raw<SG, (vm1_ < vmr_ ? vm1_ : vmr_), (full_ ? 0 : (LG))>(c); \
        if constexpr ((SG) == H1I) hop1_issue(); \
        if constexpr ((SG) == H2I) hop2_issue(); \
        if constexpr ((SG) == H1C) { hop1_check(); STAMPR(5); } \
        if constexpr ((SG) == H1C + 2) hop1_publish(); \
        if constexpr ((SG) == H2C) { hop2_check(); STAMPR(7); } \
    } while (0)
#define RD(SG, T) do { Bn0 = trfrag((T) + slot_off(SG)); Bn1 = trfrag((T) + slot_off(SG) + 4096); } while (0)
#define ROT() do { Bc0 = Bn0; Bc1 = Bn1; } while (0)

    u32 hp[8];
    u32x4 bq0, bq1;
    auto gelu2 = [&](int i) {
        const u32 bb = i < 4 ? bq0[i & 3] : bq1[i & 3];
        hp[i] = cvtpk(gelu(__builtin_fmaf(accP[2 * i], r2, bflo(bb))), gelu(__builtin_fmaf(accP[2 * i + 1], r2, bfhi(bb))));
    };
    auto bias_ld = [&](int wblk) { const u32 a = L0 + L_B1 + wblk * 64 + hf * 32; bq0 = lds_ld<u32x4>(a); bq1 = lds_ld<u32x4>(a + 16); };
    auto h_st = [&](int wblk, int half, bool live) {
        const u32 a = live ? hrow + (wblk - (s & 1)) * 64 + hf * 32 + half * 16 : L0 + L_DUMMY;
        lds_st<u32x4>(a, (u32x4){hp[4 * half], hp[4 * half + 1], hp[4 * half + 2], hp[4 * half + 3]});
    };

    SYNC(0, 0);
    STAMPR(2);
    RD(0, tKV); ROT();
    acc = zero16;
#define G1STEP(SG) do { if constexpr ((SG) % 2 == 0 && (SG) > 0) SYNC(SG, 4); \
        if constexpr ((SG) < 13) RD((SG) + 1, tKV); else RD((SG) + 1, tW1); \
        acc = MFMA32(A[2 * (SG)], Bc0, acc); acc = MFMA32(A[2 * (SG) + 1], Bc1, acc); ROT(); } while (0)
    G1STEP(0); G1STEP(1); G1STEP(2); G1STEP(3); G1STEP(4); G1STEP(5); G1STEP(6);
    G1STEP(7); G1STEP(8); G1STEP(9); G1STEP(10); G1STEP(11); G1STEP(12); G1STEP(13);
    accP = acc; acc = zero16;
    STAMPR(3);

#define W1STEP(SG, B0, TN) do { RD((SG) + 1, TN); \
        acc = MFMA32(Bc0, A[2 * ((SG) - (B0))], acc); acc = MFMA32(Bc1, A[2 * ((SG) - (B0)) + 1], acc); ROT(); } while (0)
    SYNC(14, 4);
    W1STEP(14, 14, tW1);
    {
        u32 pk[8];
        if (g < 2) {
#pragma unroll
            for (int i = 0; i < 8; ++i) { u32 v = cvtpk(phi(accP[2 * i]), phi(accP[2 * i + 1])); pk[i] = l31 < 28 ? v : 0u; }
        } else {
#pragma unroll
            for (int i = 0; i < 8; ++i) { u32 v = cvtpk(accP[2 * i], accP[2 * i + 1]); pk[i] = l31 == 28 ? 0x3f803f80u : v; }
        }
        const u32 a = L0 + L_KVX + (g * 8 + th * 4 + hf * 2) * 512 + l31 * 16;
        lds_st<u32x4>(a, (u32x4){pk[0], pk[1], pk[2], pk[3]});
        lds_st<u32x4>(a + 512, (u32x4){pk[4], pk[5], pk[6], pk[7]});
        if (w == 7) lds_st<u32x4>(L0 + L_H + lane * HS + 560, (u32x4){0u, 0u, 0u, 0u});
    }
    W1STEP(15, 14, tW1);
    SYNC(16, 4);
    {
        const u32 ka = L0 + L_KVX + ((w >> 2) * 8 + (lane >> 4)) * 512 + (16 * ((w >> 1) & 1) + (lane & 15)) * 16;
        const u32 va = L0 + L_KVX + ((2 + (w >> 2)) * 8 + (lane >> 4)) * 512 + (16 * (w & 1) + (lane & 15)) * 16;
        f32x4 kv = {0.f, 0.f, 0.f, 0.f};
        kv = MFMA16(lds_ld<bf16x8>(ka), lds_ld<bf16x8>(va), kv);
        kv = MFMA16(lds_ld<bf16x8>(ka + 2048), lds_ld<bf16x8>(va + 2048), kv);
        const u32 vo = ((4 * w + hf) * 32 + t) * 512 + l31 * 16;
        st16c(wsP, vo, (u32x4){__float_as_uint(kv[0]), tag, __float_as_uint(kv[1]), tag});
        st16c(wsP + 2 * 16384, vo, (u32x4){__float_as_uint(kv[2]), tag, __float_as_uint(kv[3]), tag});
    }
    STAMPR(4);
    W1STEP(16, 14, tW1); W1STEP(17, 14, tW1);
#define W1PAIR(SG, B0) do { SYNC(SG, 4); W1STEP(SG, B0, tW1); W1STEP((SG) + 1, B0, tW1); } while (0)
    W1PAIR(18, 14); W1PAIR(20, 14); W1PAIR(22, 14); W1PAIR(24, 14); W1PAIR(26, 14);
    accP = acc; acc = zero16;

    const int wbA = (s & 1) ? 7 - g : g, wbB = (s & 1) ? 3 - g : 4 + g;
    SYNC(28, 4); bias_ld(wbA); W1STEP(28, 28, tW1); W1STEP(29, 28, tW1); gelu2(0);
    SYNC(30, 4); W1STEP(30, 28, tW1); gelu2(1); W1STEP(31, 28, tW1);
    SYNC(32, 4); W1STEP(32, 28, tW1); gelu2(2); W1STEP(33, 28, tW1); gelu2(3);
    SYNC(34, 4); h_st(wbA, 0, true); W1STEP(34, 28, tW1); gelu2(4); W1STEP(35, 28, tW1);
    SYNC(36, 4); W1STEP(36, 28, tW1); gelu2(5); W1STEP(37, 28, tW1);
    SYNC(38, 4); W1STEP(38, 28, tW1); gelu2(6); W1STEP(39, 28, tW1);
    SYNC(40, 4); W1STEP(40, 28, tW1); gelu2(7); h_st(wbA, 1, true); W1STEP(41, 28, tQ);
    accP = acc; acc = zero16;
    STAMPR(6);

    bf16x8 Hf[18];
    u32 pq[8];
    if (g < 2) {
        SYNC(42, 4); bias_ld(wbB); gelu2(0); gelu2(1); gelu2(2);
        SYNC(44, 4); gelu2(3); h_st(wbB, 0, true); gelu2(4); gelu2(5);
        SYNC(46, 4); gelu2(6); gelu2(7); h_st(wbB, 1, true);
        SYNC(48, 4);
        RD(49, tO); ROT();
#pragma unroll
        for (int j = 0; j < 14; ++j) Hf[j] = lds_ld<bf16x8>(hrow + hf * 16 + j * 32);
#pragma unroll
        for (int i = 0; i < 8; ++i) pq[i] = 0;
    } else {
        const u32 tQ2 = tQ ^ 128u;
#define QSTEP(SG, TN) do { RD(SG, tQ2); \
        acc = MFMA32(Bc0, A[2 * ((SG) - 42)], acc); acc = MFMA32(Bc1, A[2 * ((SG) - 42) + 1], acc); ROT(); \
        RD((SG) + 1, TN); \
        acc = MFMA32(Bc0, A[14 + 2 * ((SG) - 42)], acc); acc = MFMA32(Bc1, A[15 + 2 * ((SG) - 42)], acc); ROT(); } while (0)
        SYNC(42, 4); bias_ld(wbB); QSTEP(42, tQ); gelu2(0); QSTEP(43, tQ); gelu2(1); gelu2(2);
        SYNC(44, 4); QSTEP(44, tQ); gelu2(3); h_st(wbB, 0, g == 2); gelu2(4); QSTEP(45, tQ); gelu2(5);
        SYNC(46, 4); QSTEP(46, tQ); gelu2(6); gelu2(7); h_st(wbB, 1, g == 2); QSTEP(47, tQ);
        SYNC(48, 4);
        QSTEP(48, tO);
#pragma unroll
        for (int j = 0; j < 14; ++j) Hf[j] = lds_ld<bf16x8>(hrow + hf * 16 + j * 32);
#pragma unroll
        for (int i = 0; i < 8; ++i) pq[i] = cvtpk(phi(acc[2 * i]), phi(acc[2 * i + 1]));
    }
    acc = zero16; accP = zero16;

    auto attn_o = [&]() {
        if (g >= 2) {
            const int m = l31, e = 16 * ((m >> 2) & 1) + 4 * (m >> 3) + (m & 3);
            const u32 ka = L0 + L_KVB + (g & 1) * 2560 + e * 80 + hf * 32;
            f32x16 nm = MFMA32(lds_ld<bf16x8>(ka), __builtin_bit_cast(bf16x8, (u32x4){pq[0], pq[1], pq[2], pq[3]}), zero16);
            nm = MFMA32(lds_ld<bf16x8>(ka + 16), __builtin_bit_cast(bf16x8, (u32x4){pq[4], pq[5], pq[6], pq[7]}), nm);
            auto dd = __builtin_amdgcn_permlane32_swap(__float_as_uint(nm[12]), __float_as_uint(nm[12]), false, false);
            const float rd = __builtin_amdgcn_rcpf(__uint_as_float(dd[1]) + 1e-6f);
            const u32 oa = hrow + 448 + (g & 1) * 56 + hf * 32;
#pragma unroll
            for (int i = 0; i < 4; ++i) {
                u32x2 v = {cvtpk(nm[4 * i] * rd, nm[4 * i + 1] * rd), cvtpk(nm[4 * i + 2] * rd, nm[4 * i + 3] * rd)};
                if (i < 3 || hf == 0) lds_st<u32x2>(oa + i * 8, v);
            }
        }
    };
    auto tile_st = [&](const f32x16& a, char* p0, char* p1) {
        st16c(p0, lane * 16, (u32x4){cvtpk(a[0], a[1]), cvtpk(a[2], a[3]), cvtpk(a[4], a[5]), cvtpk(a[6], a[7])});
        st16c(p1, lane * 16, (u32x4){cvtpk(a[8], a[9]), cvtpk(a[10], a[11]), cvtpk(a[12], a[13]), cvtpk(a[14], a[15])});
    };
    auto flag_st = [&](int dest, int j) { if (lane == 0) st8c(wsF + dest * 256 + j * 64 + s * 8, 0u, (u32x2){tag, (u32)(tag64 >> 32)}); };
    char* const otile = wsO + (size_t)(w * 7 * 8 + s) * 1024;
#define OSTEP(SG, K0, ACC) do { \
        if constexpr ((SG) % 2 == 0) { SYNC(SG, 4); \
            if constexpr ((SG) == H2C + 2) attn_o(); \
            if constexpr ((SG) == H2C + 4) { _Pragma("unroll") for (int j = 14; j < 18; ++j) Hf[j] = lds_ld<bf16x8>(hrow + hf * 16 + j * 32); } \
            if constexpr ((SG) == FL01) { flag_st(w, 0); flag_st(w, 1); } } \
        RD((SG) + 1, tO); \
        ACC = MFMA32(Bc0, Hf[K0], ACC); ACC = MFMA32(Bc1, Hf[(K0) + 1], ACC); ROT(); } while (0)
    static_assert(H2C + 4 <= 62 && H2C >= 50 && H2I >= H1C + 4 && H1I >= 18, "schedule");
    OSTEP(49, 0, acc); OSTEP(50, 2, acc); OSTEP(51, 4, acc); OSTEP(52, 6, acc); OSTEP(53, 8, acc); OSTEP(54, 10, acc); OSTEP(55, 12, acc);
    OSTEP(56, 0, accP); OSTEP(57, 2, accP); OSTEP(58, 4, accP); OSTEP(59, 6, accP); OSTEP(60, 8, accP); OSTEP(61, 10, accP); OSTEP(62, 12, accP);
    STAMPR(8);
    OSTEP(63, 14, acc); OSTEP(64, 16, acc);
    OSTEP(65, 14, accP);
    tile_st(acc, otile, otile + 8192); acc = zero16;
    OSTEP(66, 16, accP);
    OSTEP(67, 0, acc);
    tile_st(accP, otile + 2 * 8192, otile + 3 * 8192);
    OSTEP(68, 2, acc); OSTEP(69, 4, acc); OSTEP(70, 6, acc); OSTEP(71, 8, acc); OSTEP(72, 10, acc); OSTEP(73, 12, acc);
    OSTEP(74, 14, acc); OSTEP(75, 16, acc);
    sync_raw<76, (FL01 == 74 ? 2 : 0), 0>(c);
    STAMPR(9);
    tile_st(acc, otile + 4 * 8192, otile + 5 * 8192);

    auto finish_unit = [&](int u) {
        const int j = u >> 1;
        int tk, col;
        if (u < 6) { tk = 32 * (s & 1) + l31; col = 128 * j + 32 * (s >> 1) + 16 * hf + 8 * (u & 1); }
        else { tk = 32 * ((s >> 1) & 1) + l31; col = 384 + 32 * (s >> 2) + 16 * hf + 8 * (s & 1); }
        const size_t eo = (size_t)(64 * t + tk) * DM + col;
        const u32x4 xv = *(const GAS u32x4*)(x + eo);
        const u32x4 bv = *(const GAS u32x4*)(b2 + col);
        const GAS u64* fl = (const GAS u64*)(wsF + s * 256 + j * 64) + (lane & 7);
        for (u32 spin = 0; spin < SPIN_CAP; ++spin) {
            const u64 f = __hip_atomic_load(fl, __ATOMIC_RELAXED, __HIP_MEMORY_SCOPE_AGENT);
            if (__all(f == tag64)) break;
        }
        const char* src = wsO + (size_t)((s * 7 + u) * 8) * 1024;
        u32x4 pv[8];
        const u32 vo = lane * 16;
        asm volatile("global_load_dwordx4 %0, %8, %9 sc1\n\tglobal_load_dwordx4 %1, %8, %9 offset:1024 sc1\n\t"
                     "global_load_dwordx4 %2, %8, %9 offset:2048 sc1\n\tglobal_load_dwordx4 %3, %8, %9 offset:3072 sc1\n\t"
                     "global_load_dwordx4 %4, %8, %10 sc1\n\tglobal_load_dwordx4 %5, %8, %10 offset:1024 sc1\n\t"
                     "global_load_dwordx4 %6, %8, %10 offset:2048 sc1\n\tglobal_load_dwordx4 %7, %8, %10 offset:3072 sc1\n\t"
                     "s_waitcnt vmcnt(0)"
                     : "=&v"(pv[0]), "=&v"(pv[1]), "=&v"(pv[2]), "=&v"(pv[3]), "=&v"(pv[4]), "=&v"(pv[5]), "=&v"(pv[6]), "=&v"(pv[7])
                     : "v"(vo), "s"(src), "s"(src + 4096) : "memory");
        u32x4 ov;
#pragma unroll
        for (int e = 0; e < 4; ++e) {
            float lo = bflo(pv[0][e]), hi = bfhi(pv[0][e]);
#pragma unroll
            for (int i = 1; i < 8; ++i) { lo += bflo(pv[i][e]); hi += bfhi(pv[i][e]); }
            ov[e] = cvtpk(lo + bflo(xv[e]) + bflo(bv[e]), hi + bfhi(xv[e]) + bfhi(bv[e]));
        }
        *(GAS u32x4*)(out + eo) = ov;
    };

    if (g < 2) {
        const u32 tO2 = tO ^ 128u;
        acc = zero16;
#define LSTEP(SG, K0) do { RD(SG, tO2); \
        acc = MFMA32(Bc0, Hf[K0], acc); acc = MFMA32(Bc1, Hf[(K0) + 1], acc); ROT(); \
        if constexpr ((SG) < 80) RD((SG) + 1, tO); \
        acc = MFMA32(Bc0, Hf[(K0) + 2], acc); acc = MFMA32(Bc1, Hf[(K0) + 3], acc); ROT(); } while (0)
        LSTEP(76, 0); LSTEP(77, 4); LSTEP(78, 8);
        RD(80, tO); acc = MFMA32(Bc0, Hf[12], acc); acc = MFMA32(Bc1, Hf[13], acc); ROT();
        LSTEP(80, 14);
        char* o6 = wsO + (size_t)(((2 * w) * 7 + 6) * 8 + s) * 1024;
        tile_st(acc, o6, o6 + 7 * 8192);
        STAMPR(10);
        waitvm<2>(); flag_st(w, 2);
        waitvm<1>(); flag_st(2 * w, 3); flag_st(2 * w + 1, 3);
        STAMPR(11);
    } else {
        waitvm<0>(); flag_st(w, 2);
        STAMPR(10);
        finish_unit(w - 4);
        STAMPR(11);
        if (w < 7) finish_unit(w);
        else if (blockIdx.x == 0 && lane == 0) st8c(ws, 0u, (u32x2){tag, (u32)(tag64 >> 32)});
    }
    STAMPR(12);
}

extern "C" void kernel_launch(void* const* d_in, const int* in_sizes, int n_in, void* d_out, int out_size, void* d_ws, size_t ws_size,
                              hipStream_t stream) {
    (void)in_sizes; (void)n_in; (void)out_size; (void)ws_size;
    (void)hipFuncSetAttribute((const void*)fused_block, hipFuncAttributeMaxDynamicSharedMemorySize, LDS_BYTES);
    hipLaunchKernelGGL(fused_block, dim3(256), dim3(512), LDS_BYTES, stream,
                       (const u16*)d_in[0], (const u16*)d_in[1], (const u16*)d_in[2], (const u16*)d_in[4], (const u16*)d_in[5],
                       (const u16*)d_in[6], (const u16*)d_in[7], (u16*)d_out, (char*)d_ws);
}
```

```cpp
#define DIAG
#define HDIAG
#include <stdio.h>
#pragma clang diagnostic ignored "-Wint-to-pointer-cast"
#include <hip/hip_runtime.h>
#include <stdint.h>

typedef unsigned short u16;
typedef unsigned u32;
typedef unsigned long long u64;
using bf16x8 = __attribute__((ext_vector_type(8))) short;
using s16x4 = __attribute__((ext_vector_type(4))) short;
using f32x16 = __attribute__((ext_vector_type(16))) float;
using f32x4 = __attribute__((ext_vector_type(4))) float;
using f32x2 = __attribute__((ext_vector_type(2))) float;
using u32x4 = __attribute__((ext_vector_type(4))) unsigned;
using u32x2 = __attribute__((ext_vector_type(2))) unsigned;
#define LAS __attribute__((address_space(3)))
#define GAS __attribute__((address_space(1)))
#define DEV __device__ __forceinline__

#define BSL 4
#define H1I 30
#define H1C 36
#define H2I 48
#define H2C 54
#define FL01 74

namespace {
constexpr int DM = 448, FF = 1792;
constexpr int NSLAB = 81, RING = 12, SLABB = 8192;
constexpr int L_XN = RING * SLABB;
constexpr int XS = 912, HS = 592;
constexpr int L_H = L_XN;
constexpr int L_KVX = L_XN + 64 * HS;
constexpr int L_RED = L_KVX + 16384;
constexpr int L_MISC = L_XN + 64 * XS;
constexpr int L_R2 = L_MISC;
constexpr int L_B1 = L_MISC + 256;
constexpr int L_KVB = L_B1 + 1024;
constexpr int L_DUMMY = L_KVB + 5120;
constexpr int LDS_BYTES = L_DUMMY + 256;
static_assert(L_RED + 2048 <= L_MISC, "lds");
static_assert(LDS_BYTES <= 163840, "lds");
constexpr size_t W_E = 0, W_FL = 4096, W_P = 0x20000, W_R = W_P + (4u << 20), W_O = W_R + 65536;
constexpr size_t W_TS = 32u << 20;
constexpr u32 SPIN_CAP = 1u << 18;

typedef __bf16 bf16x2_t __attribute__((ext_vector_type(2)));
DEV u32 cvtpk(float lo, float hi) { f32x2 v = {lo, hi}; return __builtin_bit_cast(u32, __builtin_convertvector(v, bf16x2_t)); }
DEV float bflo(u32 v) { return __uint_as_float(v << 16); }
DEV float bfhi(u32 v) { return __uint_as_float(v & 0xffff0000u); }
template <int CTRL> DEV float dpp(float v) { return __int_as_float(__builtin_amdgcn_update_dpp(0, __float_as_int(v), CTRL, 0xf, 0xf, true)); }
template <int IMM> DEV void glds16(const char* sb, u32 voff, u32 ldsw) {
    asm volatile("s_add_u32 m0, %2, %3\n\ts_nop 0\n\tglobal_load_lds_dwordx4 %0, %1" :: "v"(voff), "s"(sb), "s"(ldsw), "n"(IMM) : "memory", "scc");
}
template <int IMM> DEV void glds16c(const char* sb, u32 voff, u32 ldsw) {
    asm volatile("s_add_u32 m0, %2, %3\n\ts_nop 0\n\tglobal_load_lds_dwordx4 %0, %1 sc1" :: "v"(voff), "s"(sb), "s"(ldsw), "n"(IMM) : "memory", "scc");
}
DEV void touch(const char* sb, u32 voff, u32 lds) {
    asm volatile("s_mov_b32 m0, %2\n\ts_nop 0\n\tglobal_load_lds_dword %0, %1" :: "v"(voff), "s"(sb), "s"(lds) : "memory");
}
DEV void st16c(char* sb, u32 voff, u32x4 v) { asm volatile("global_store_dwordx4 %0, %1, %2 sc1\n\ts_nop 1" :: "v"(voff), "v"(v), "s"(sb) : "memory"); }
DEV void st8c(char* sb, u32 voff, u32x2 v) { asm volatile("global_store_dwordx2 %0, %1, %2 sc1" :: "v"(voff), "v"(v), "s"(sb) : "memory"); }
template <int N> DEV void waitvm() { asm volatile("s_waitcnt vmcnt(%0)" :: "n"(N) : "memory"); }
template <typename T> DEV T lds_ld(u32 a) { return *(const LAS T*)(a); }
template <typename T> DEV void lds_st(u32 a, T v) { *(LAS T*)(a) = v; }
typedef short v4i16_t __attribute__((ext_vector_type(4)));
DEV bf16x8 trfrag(u32 a) {
    s16x4 lo = __builtin_amdgcn_ds_read_tr16_b64_v4i16((LAS v4i16_t*)(a));
    s16x4 hi = __builtin_amdgcn_ds_read_tr16_b64_v4i16((LAS v4i16_t*)(a + 1024));
    return __builtin_shufflevector(lo, hi, 0, 1, 2, 3, 4, 5, 6, 7);
}
#define MFMA32(a, b, c) __builtin_amdgcn_mfma_f32_32x32x16_bf16(a, b, c, 0, 0, 0)
#define MFMA16(a, b, c) __builtin_amdgcn_mfma_f32_16x16x32_bf16(a, b, c, 0, 0, 0)
#define SBAR() __builtin_amdgcn_sched_barrier(0)

DEV float phi(float t) { float e = __builtin_amdgcn_exp2f(t * 1.4426950408889634f); return (t > 0.f ? t + 1.f : e) + 1e-6f; }
DEV float gelu(float u) {
    const float k0 = -2.f * 0.7978845608028654f * 1.4426950408889634f, k1 = k0 * 0.044715f;
    float p = __builtin_fmaf(u * u, k1, k0);
    float e = __builtin_amdgcn_exp2f(u * p);
    return u * __builtin_amdgcn_rcpf(1.f + e);
}

constexpr int nglds(int s) { return s < 2 ? 0 : ((s + 10 < NSLAB) + (s + 11 < NSLAB)); }
constexpr int extra(int s) { return (s == 16 ? 2 : 0) + (s == H1I ? 2 : 0) + (s == H2I ? 1 : 0) + (s == 64 ? 2 : 0) + (s == 66 ? 2 : 0) + (s == FL01 ? 2 : 0); }
constexpr int younger_than_extra(int from, int upto) { int n = 0; for (int i = from + 2; i < upto; i += 2) n += nglds(i) + extra(i); return n; }
constexpr int vmwait(int s) {
    if (s + 3 <= 11) { int n = 11 - (s + 3); for (int i = 0; i < s; i += 2) n += nglds(i) + extra(i); return n; }
    int last = (s + 3 < NSLAB) ? s - 8 : 70;
    return extra(last) + younger_than_extra(last, s);
}

struct Ctx {
    const char *gKV, *gW1a, *gW1b, *gQ, *gW2, *gWo;
    u32 vKV, vQ, vW1, vW2, vWo1, vL, vLd, vLo;
    u32 ldsw;
};
template <int SG> DEV void issue(const Ctx& c) {
    constexpr int IMM = (SG % RING) * SLABB;
    if constexpr (SG < 14) glds16<IMM>(c.gKV + SG * 32 * 2688, c.vKV, c.ldsw);
    else if constexpr (SG < 28) glds16<IMM>(c.gW1a + (SG - 14) * 32 * 3584, c.vW1, c.ldsw);
    else if constexpr (SG < 42) glds16<IMM>(c.gW1b + (SG - 28) * 32 * 3584, c.vW1, c.ldsw);
    else if constexpr (SG < 49) glds16<IMM>(c.gQ + (SG - 42) * 32 * 2688, c.vQ, c.ldsw);
    else if constexpr (SG < 56) glds16<IMM>(c.gW2 + (SG - 49) * 32 * 896, c.vW2, c.ldsw);
    else if constexpr (SG < 63) glds16<IMM>(c.gW2 + (SG - 56) * 32 * 896 + 256, c.vW2, c.ldsw);
    else if constexpr (SG == 63) glds16<IMM>(c.gWo, c.vW2, c.ldsw);
    else if constexpr (SG == 64) glds16<IMM>(c.gWo, c.vWo1, c.ldsw);
    else if constexpr (SG == 65) glds16<IMM>(c.gWo + 256, c.vW2, c.ldsw);
    else if constexpr (SG == 66) glds16<IMM>(c.gWo + 256, c.vWo1, c.ldsw);
    else if constexpr (SG < 74) glds16<IMM>(c.gW2 + (SG - 67) * 32 * 896 + 512, c.vW2, c.ldsw);
    else if constexpr (SG == 74) glds16<IMM>(c.gWo + 512, c.vW2, c.ldsw);
    else if constexpr (SG == 75) glds16<IMM>(c.gWo + 512, c.vWo1, c.ldsw);
    else if constexpr (SG < 79) glds16<IMM>(c.gW2 + (SG - 76) * 64 * 896 + 768, c.vL, c.ldsw);
    else if constexpr (SG == 79) glds16<IMM>(c.gW2 + 192 * 896 + 768, c.vLd, c.ldsw);
    else glds16<IMM>(c.gWo + 768, c.vLo, c.ldsw);
}
template <int SG, int VM, int LG> DEV void sync_raw(const Ctx& c) {
    SBAR();
    asm volatile("s_waitcnt vmcnt(%0) lgkmcnt(%1)\n\ts_barrier" :: "n"(VM), "n"(LG) : "memory");
    if constexpr (SG >= 2 && SG + 10 < NSLAB) issue<SG + 10>(c);
    if constexpr (SG >= 2 && SG + 11 < NSLAB) issue<SG + 11>(c);
    SBAR();
}
constexpr u32 slot_off(int sg) { return (u32)((sg % RING) * SLABB); }
}

#define STAMP(i) do { if (lane == 0) { SBAR(); ((GAS u64*)(ws + W_TS))[((size_t)blockIdx.x * 8 + w) * 16 + (i)] = __builtin_amdgcn_s_memrealtime(); SBAR(); } } while (0)
#define STAMPR(i) do { SBAR(); asm volatile("s_memrealtime %0\n\ts_waitcnt lgkmcnt(0)" : "=s"(tsv[i]) :: "memory"); SBAR(); } while (0)

__global__ __launch_bounds__(512, 2) void fused_block(const u16* __restrict__ x, const u16* __restrict__ wqkv, const u16* __restrict__ wo,
                                                      const u16* __restrict__ w1, const u16* __restrict__ b1, const u16* __restrict__ w2,
                                                      const u16* __restrict__ b2, u16* __restrict__ out, char* __restrict__ ws) {
    constexpr u32 L0 = 0;
    asm volatile("" :: "s"(x), "s"(wqkv), "s"(wo), "s"(w1), "s"(b1), "s"(w2), "s"(b2), "s"(out), "s"(ws));
    const int lane = threadIdx.x & 63;
    const int w = __builtin_amdgcn_readfirstlane(threadIdx.x >> 6);
    const int th = w & 1, g = w >> 1;
    const int xcd = blockIdx.x & 7, jj = blockIdx.x >> 3;
    const int s = (xcd % (8 / BSL)) * BSL + (jj % BSL);
    const int t = (xcd / (8 / BSL)) * (32 / BSL) + jj / BSL;
    u64 tsv[16];
    for (int i = 0; i < 16; ++i) tsv[i] = 0;
    STAMPR(0);
    u64 cy0; asm volatile("s_memtime %0\n\ts_waitcnt lgkmcnt(0)" : "=s"(cy0) :: "memory");

    u32x4 xr[7];
    const int xrow = 8 * w + (lane >> 3);
    const int rot = (jj % BSL) * (8 / BSL);
    {
        const char* xb = (const char*)x + (size_t)t * (64 * 896);
        const u32 vx = xrow * 896 + (lane & 7) * 16;
#pragma unroll
        for (int j = 0; j < 7; ++j) {
            int jr = j + rot; jr = jr >= 7 ? jr - 7 : jr;
            asm volatile("global_load_dwordx4 %0, %1, %2" : "=v"(xr[j]) : "v"(vx), "s"(xb + jr * 128));
        }
    }
    Ctx c;
    {
        const int r = lane >> 4, ch = (lane & 15) ^ (r << 2), seg = ch >> 3, c8 = ch & 7, row = 4 * w + r;
        const int c7 = c8 > 6 ? 6 : c8;
        c.vKV = row * 2688 + seg * 896 + c7 * 16;
        c.vQ = (row + seg * 224) * 2688 + c7 * 16;
        c.vW1 = row * 3584 + ch * 16;
        c.vW2 = row * 896 + ch * 16;
        c.vWo1 = (row + 32 > 55 ? 55 : row + 32) * 896 + ch * 16;
        c.vL = (row + 32 * seg) * 896 + c8 * 16;
        c.vLd = row * 896 + c8 * 16;
        c.vLo = (row + 32 * seg > 55 ? 55 : row + 32 * seg) * 896 + c8 * 16;
        c.gKV = (const char*)wqkv + 896 + 112 * s;
        c.gQ = (const char*)wqkv + 112 * s;
        const int wb = 448 * s - 64 * (s & 1);
        c.gW1a = (const char*)w1 + wb + ((s & 1) ? 256 : 0);
        c.gW1b = (const char*)w1 + wb + ((s & 1) ? 0 : 256);
        c.gW2 = (const char*)w2 + (size_t)s * (224 * 896);
        c.gWo = (const char*)wo + (size_t)s * (56 * 896);
        c.ldsw = L0 + w * 1024;
    }
    constexpr int NPF = 2;
    {
        const int s0 = s - (jj % BSL);
        const int tid = threadIdx.x;
        {
            constexpr int LPS = (BSL == 1) ? 2 : (BSL == 2 ? 3 : 4);
            const int base = (112 * s0) & ~127;
            int i = tid; const int n = 14 * 3 * LPS;
            u32 off;
            const char* sb = (const char*)wqkv;
            i = i < n ? i : n - 1;
            const int m = i / (14 * LPS), rem = i % (14 * LPS), rr = rem / LPS, ll = rem % LPS;
            const int mm = m == 2 ? 0 : m + 1;
            int col = base + ll * 128; col = col > 768 ? 768 : col;
            off = (14 * jj + rr) * 2688 + mm * 896 + col;
            if (w * 64 < n) touch(sb, off, L0 + L_DUMMY);
            else {
                constexpr int LW = (BSL == 1) ? 4 : (7 * BSL / 2);
                constexpr int W0 = (14 * 3 * LPS + 63) / 64;
                int k = tid - W0 * 64; const int n1 = 14 * LW;
                k = k < n1 ? k : n1 - 1;
                const int bw = (448 * s0) & ~127;
                touch((const char*)w1, (14 * jj + k / LW) * 3584 + bw + (k % LW) * 128, L0 + L_DUMMY);
            }
        }
        {
            constexpr int N2 = 49 * BSL, NO = (392 * BSL + 31) / 32;
            if (w == 7) {
                const int wb = 448 * s - 64 * (s & 1);
                glds16<L_B1>((const char*)b1 + wb, (lane & 31) * 16, L0);
            } else if (w >= 5) {
                int k = jj * NO + (tid - 320 < NO ? tid - 320 : NO - 1); k = k < 392 * BSL ? k : 392 * BSL - 1;
                touch((const char*)wo + (size_t)s0 * (56 * 896), k * 128, L0 + L_DUMMY);
            } else {
                int k = tid < N2 ? tid : N2 - 1;
                touch((const char*)w2 + (size_t)s0 * (224 * 896), (jj * N2 + k) * 128, L0 + L_DUMMY);
            }
        }
    }
    issue<0>(c); issue<1>(c); issue<2>(c); issue<3>(c); issue<4>(c); issue<5>(c);
    issue<6>(c); issue<7>(c); issue<8>(c); issue<9>(c); issue<10>(c); issue<11>(c);
    u64 epoch;
    asm volatile("s_load_dwordx2 %0, %1, 0x0" : "=s"(epoch) : "s"(ws));

    asm volatile("s_waitcnt vmcnt(%8) lgkmcnt(0)" : "+v"(xr[0]), "+v"(xr[1]), "+v"(xr[2]), "+v"(xr[3]), "+v"(xr[4]), "+v"(xr[5]), "+v"(xr[6]), "+s"(epoch) : "n"(12 + NPF));
    STAMPR(1);
    const u64 tag64 = epoch + 0x100000001ull;
    const u32 tag = (u32)tag64;
    {
        float s1 = 0.f, s2 = 0.f, s1b = 0.f, s2b = 0.f;
#pragma unroll
        for (int j = 0; j < 7; ++j)
#pragma unroll
            for (int e = 0; e < 4; ++e) {
                float a = bflo(xr[j][e]), b = bfhi(xr[j][e]);
                s1 += a; s2 = __builtin_fmaf(a, a, s2); s1b += b; s2b = __builtin_fmaf(b, b, s2b);
            }
        s1 += s1b; s2 += s2b;
        s1 += dpp<0xB1>(s1); s2 += dpp<0xB1>(s2);
        s1 += dpp<0x4E>(s1); s2 += dpp<0x4E>(s2);
        s1 += dpp<0x141>(s1); s2 += dpp<0x141>(s2);
        const float mean = s1 * (1.f / DM);
        const float var = __builtin_fmaxf(__builtin_fmaf(-mean, mean, s2 * (1.f / DM)), 0.f);
        const float rstd = __builtin_amdgcn_rsqf(var + 1e-5f);
        const float nb = -mean * rstd;
        const u32 xa = L0 + L_XN + xrow * XS + (lane & 7) * 16;
#pragma unroll
        for (int j = 0; j < 7; ++j) {
            int jr = j + rot; jr = jr >= 7 ? jr - 7 : jr;
            u32x4 o;
#pragma unroll
            for (int e = 0; e < 4; ++e) o[e] = cvtpk(__builtin_fmaf(bflo(xr[j][e]), rstd, nb), __builtin_fmaf(bfhi(xr[j][e]), rstd, nb));
            lds_st<u32x4>(xa + jr * 128, o);
        }
        if ((lane & 7) == 0) lds_st<float>(L0 + L_R2 + xrow * 4, __builtin_amdgcn_rsqf(var * rstd * rstd + 1e-5f));
    }
    asm volatile("s_waitcnt lgkmcnt(0)\n\ts_barrier" ::: "memory");
    const int l31 = lane & 31, hf = lane >> 5;
    bf16x8 A[28];
    {
        const u32 a = L0 + L_XN + (32 * th + l31) * XS + hf * 16;
#pragma unroll
        for (int j = 0; j < 28; ++j) A[j] = lds_ld<bf16x8>(a + j * 32);
    }
    const float r2 = lds_ld<float>(L0 + L_R2 + (32 * th + l31) * 4);

    const int blk = (lane >> 4) & 1, q = (lane & 15) >> 2, p = lane & 3;
    const u32 trrow = L0 + (8 * hf + q) * 256;
    const u32 mstd = 32 * blk + 8 * p, mperm = 32 * (p & 1) + 16 * blk + 8 * (p >> 1);
    const u32 tKV = trrow + ((((g >> 1) * 128 + (g & 1) * 56) + mstd) ^ (q << 6));
    const int gq = (s & 1) ? 3 - g : g;
    const u32 tW1 = trrow + ((64 * gq + mperm) ^ (q << 6));
    const u32 tQ = trrow + (((g & 1) * 56 + mperm) ^ (q << 6));
    const u32 tO = trrow + ((64 * g + mperm) ^ (q << 6));
    const u32 hrow = L0 + L_H + (32 * th + l31) * HS;

    char* const wsP = ws + W_P + (size_t)s * (32 * 32 * 512);
    char* const wsR = ws + W_R + (size_t)s * 8192;
    char* const wsO = ws + W_O + (size_t)t * (8 * 7 * 8 * 1024);
    char* const wsF = ws + W_FL + (size_t)t * (8 * 256);

    bf16x8 Bc0, Bc1, Bn0, Bn1;
    f32x16 acc, accP;
    const f32x16 zero16 = {0.f, 0.f, 0.f, 0.f, 0.f, 0.f, 0.f, 0.f, 0.f, 0.f, 0.f, 0.f, 0.f, 0.f, 0.f, 0.f};

    auto hop1_issue = [&]() { const char* src = wsP + (size_t)t * 16384 + w * 2048; glds16c<L_KVX>(src, lane * 16, L0 + w * 2048); glds16c<L_KVX + 1024>(src + 1024, lane * 16, L0 + w * 2048); };
    auto hop1_check = [&]() {
        u32x4 a, b;
        for (u32 spin = 0;; ++spin) {
            a = lds_ld<u32x4>(L0 + L_KVX + w * 2048 + lane * 16); b = lds_ld<u32x4>(L0 + L_KVX + w * 2048 + 1024 + lane * 16);
            const bool ok = a[1] == tag && a[3] == tag && b[1] == tag && b[3] == tag;
            if (__all(ok) || spin >= SPIN_CAP) break;
            hop1_issue(); waitvm<0>();
        }
        float v0 = __uint_as_float(a[0]) + __uint_as_float(b[0]), v1 = __uint_as_float(a[2]) + __uint_as_float(b[2]);
        auto r0 = __builtin_amdgcn_permlane32_swap(__float_as_uint(v0), __float_as_uint(v0), false, false);
        auto r1 = __builtin_amdgcn_permlane32_swap(__float_as_uint(v1), __float_as_uint(v1), false, false);
        f32x2 o = {__uint_as_float(r0[0]) + __uint_as_float(r0[1]), __uint_as_float(r1[0]) + __uint_as_float(r1[1])};
        if (lane < 32) lds_st<f32x2>(L0 + L_RED + w * 256 + lane * 8, o);
    };
    auto hop1_publish = [&]() {
        if (w == 6) {
            f32x2 v = lds_ld<f32x2>(L0 + L_RED + l31 * 8);
#pragma unroll
            for (int i = 1; i < 8; ++i) v += lds_ld<f32x2>(L0 + L_RED + i * 256 + l31 * 8);
            if (lane < 32) st8c(wsR + t * 256, lane * 8, (u32x2){cvtpk(v[0], v[1]), tag});
        }
    };
    auto hop2_issue = [&]() { glds16c<L_KVX>(wsR + w * 1024, lane * 16, L0 + w * 2048); };
    auto hop2_check = [&]() {
        u32x4 a;
        for (u32 spin = 0;; ++spin) {
            a = lds_ld<u32x4>(L0 + L_KVX + w * 2048 + lane * 16);
            const bool ok = a[1] == tag && a[3] == tag;
            if (__all(ok) || spin >= SPIN_CAP) break;
            hop2_issue(); waitvm<0>();
        }
        const int Ln = 32 * ((lane >> 4) & 1) + 2 * (lane & 15);
        const u32 ad = L0 + L_KVB + (w >> 2) * 2560 + (16 * (w & 1) + (Ln & 15)) * 80 + (16 * ((w >> 1) & 1) + 4 * (Ln >> 4) + 2 * hf) * 2;
        lds_st<u32>(ad, a[0]); lds_st<u32>(ad + 80, a[2]);
    };
#define SYNC(SG, LG) do { \
        constexpr bool full_ = ((SG) == 16 || (SG) == H1C + 2 || (SG) == 48 || (SG) == H2C + 2 || (SG) == H2C + 4); \
        constexpr int vmr_ = vmwait(SG); \
        constexpr int vm1_ = (SG) == H1C ? younger_than_extra(H1I, SG) : ((SG) == H2C ? younger_than_extra(H2I, SG) : ((SG) == FL01 ? younger_than_extra(66, SG) : 63)); \
        sync_raw<SG, (vm1_ < vmr_ ? vm1_ : vmr_), (full_ ? 0 : (LG))>(c); \
        if constexpr ((SG) == H1I) hop1_issue(); \
        if constexpr ((SG) == H2I) hop2_issue(); \
        if constexpr ((SG) == H1C) { hop1_check(); STAMPR(5); } \
        if constexpr ((SG) == H1C + 2) hop1_publish(); \
        if constexpr ((SG) == H2C) { hop2_check(); STAMPR(7); } \
    } while (0)
#define RD(SG, T) do { Bn0 = trfrag((T) + slot_off(SG)); Bn1 = trfrag((T) + slot_off(SG) + 4096); } while (0)
#define ROT() do { Bc0 = Bn0; Bc1 = Bn1; } while (0)

    u32 hp[8];
    u32x4 bq0, bq1;
    auto gelu2 = [&](int i) {
        const u32 bb = i < 4 ? bq0[i & 3] : bq1[i & 3];
        hp[i] = cvtpk(gelu(__builtin_fmaf(accP[2 * i], r2, bflo(bb))), gelu(__builtin_fmaf(accP[2 * i + 1], r2, bfhi(bb))));
    };
    auto bias_ld = [&](int wblk) { const u32 a = L0 + L_B1 + wblk * 64 + hf * 32; bq0 = lds_ld<u32x4>(a); bq1 = lds_ld<u32x4>(a + 16); };
    auto h_st = [&](int wblk, int half, bool live) {
        const u32 a = live ? hrow + (wblk - (s & 1)) * 64 + hf * 32 + half * 16 : L0 + L_DUMMY;
        lds_st<u32x4>(a, (u32x4){hp[4 * half], hp[4 * half + 1], hp[4 * half + 2], hp[4 * half + 3]});
    };

    SYNC(0, 0);
    STAMPR(2);
    RD(0, tKV); ROT();
    acc = zero16;
#define G1STEP(SG) do { if constexpr ((SG) % 2 == 0 && (SG) > 0) SYNC(SG, 4); \
        if constexpr ((SG) < 13) RD((SG) + 1, tKV); else RD((SG) + 1, tW1); \
        acc = MFMA32(A[2 * (SG)], Bc0, acc); acc = MFMA32(A[2 * (SG) + 1], Bc1, acc); ROT(); } while (0)
    G1STEP(0); G1STEP(1); G1STEP(2); G1STEP(3); G1STEP(4); G1STEP(5); G1STEP(6);
    G1STEP(7); G1STEP(8); G1STEP(9); G1STEP(10); G1STEP(11); G1STEP(12); G1STEP(13);
    accP = acc; acc = zero16;
    STAMPR(3);

#define W1STEP(SG, B0, TN) do { RD((SG) + 1, TN); \
        acc = MFMA32(Bc0, A[2 * ((SG) - (B0))], acc); acc = MFMA32(Bc1, A[2 * ((SG) - (B0)) + 1], acc); ROT(); } while (0)
    SYNC(14, 4);
    W1STEP(14, 14, tW1);
    {
        u32 pk[8];
        if (g < 2) {
#pragma unroll
            for (int i = 0; i < 8; ++i) { u32 v = cvtpk(phi(accP[2 * i]), phi(accP[2 * i + 1])); pk[i] = l31 < 28 ? v : 0u; }
        } else {
#pragma unroll
            for (int i = 0; i < 8; ++i) { u32 v = cvtpk(accP[2 * i], accP[2 * i + 1]); pk[i] = l31 == 28 ? 0x3f803f80u : v; }
        }
        const u32 a = L0 + L_KVX + (g * 8 + th * 4 + hf * 2) * 512 + l31 * 16;
        lds_st<u32x4>(a, (u32x4){pk[0], pk[1], pk[2], pk[3]});
        lds_st<u32x4>(a + 512, (u32x4){pk[4], pk[5], pk[6], pk[7]});
        if (w == 7) lds_st<u32x4>(L0 + L_H + lane * HS + 560, (u32x4){0u, 0u, 0u, 0u});
    }
    W1STEP(15, 14, tW1);
    SYNC(16, 4);
    {
        const u32 ka = L0 + L_KVX + ((w >> 2) * 8 + (lane >> 4)) * 512 + (16 * ((w >> 1) & 1) + (lane & 15)) * 16;
        const u32 va = L0 + L_KVX + ((2 + (w >> 2)) * 8 + (lane >> 4)) * 512 + (16 * (w & 1) + (lane & 15)) * 16;
        f32x4 kv = {0.f, 0.f, 0.f, 0.f};
        kv = MFMA16(lds_ld<bf16x8>(ka), lds_ld<bf16x8>(va), kv);
        kv = MFMA16(lds_ld<bf16x8>(ka + 2048), lds_ld<bf16x8>(va + 2048), kv);
        const u32 vo = ((4 * w + hf) * 32 + t) * 512 + l31 * 16;
        st16c(wsP, vo, (u32x4){__float_as_uint(kv[0]), tag, __float_as_uint(kv[1]), tag});
        st16c(wsP + 2 * 16384, vo, (u32x4){__float_as_uint(kv[2]), tag, __float_as_uint(kv[3]), tag});
    }
    STAMPR(4);
    W1STEP(16, 14, tW1); W1STEP(17, 14, tW1);
#define W1PAIR(SG, B0) do { SYNC(SG, 4); W1STEP(SG, B0, tW1); W1STEP((SG) + 1, B0, tW1); } while (0)
    W1PAIR(18, 14); W1PAIR(20, 14); W1PAIR(22, 14); W1PAIR(24, 14); W1PAIR(26, 14);
    accP = acc; acc = zero16;

    const int wbA = (s & 1) ? 7 - g : g, wbB = (s & 1) ? 3 - g : 4 + g;
    SYNC(28, 4); bias_ld(wbA); W1STEP(28, 28, tW1); W1STEP(29, 28, tW1); gelu2(0);
    SYNC(30, 4); W1STEP(30, 28, tW1); gelu2(1); W1STEP(31, 28, tW1);
    SYNC(32, 4); W1STEP(32, 28, tW1); gelu2(2); W1STEP(33, 28, tW1); gelu2(3);
    SYNC(34, 4); h_st(wbA, 0, true); W1STEP(34, 28, tW1); gelu2(4); W1STEP(35, 28, tW1);
    SYNC(36, 4); W1STEP(36, 28, tW1); gelu2(5); W1STEP(37, 28, tW1);
    SYNC(38, 4); W1STEP(38, 28, tW1); gelu2(6); W1STEP(39, 28, tW1);
    SYNC(40, 4); W1STEP(40, 28, tW1); gelu2(7); h_st(wbA, 1, true); W1STEP(41, 28, tQ);
    accP = acc; acc = zero16;
    STAMPR(6);

    bf16x8 Hf[18];
    u32 pq[8];
    if (g < 2) {
        SYNC(42, 4); bias_ld(wbB); gelu2(0); gelu2(1); gelu2(2);
        SYNC(44, 4); gelu2(3); h_st(wbB, 0, true); gelu2(4); gelu2(5);
        SYNC(46, 4); gelu2(6); gelu2(7); h_st(wbB, 1, true);
        SYNC(48, 4);
        RD(49, tO); ROT();
#pragma unroll
        for (int j = 0; j < 14; ++j) Hf[j] = lds_ld<bf16x8>(hrow + hf * 16 + j * 32);
#pragma unroll
        for (int i = 0; i < 8; ++i) pq[i] = 0;
    } else {
        const u32 tQ2 = tQ ^ 128u;
#define QSTEP(SG, TN) do { RD(SG, tQ2); \
        acc = MFMA32(Bc0, A[2 * ((SG) - 42)], acc); acc = MFMA32(Bc1, A[2 * ((SG) - 42) + 1], acc); ROT(); \
        RD((SG) + 1, TN); \
        acc = MFMA32(Bc0, A[14 + 2 * ((SG) - 42)], acc); acc = MFMA32(Bc1, A[15 + 2 * ((SG) - 42)], acc); ROT(); } while (0)
        SYNC(42, 4); bias_ld(wbB); QSTEP(42, tQ); gelu2(0); QSTEP(43, tQ); gelu2(1); gelu2(2);
        SYNC(44, 4); QSTEP(44, tQ); gelu2(3); h_st(wbB, 0, g == 2); gelu2(4); QSTEP(45, tQ); gelu2(5);
        SYNC(46, 4); QSTEP(46, tQ); gelu2(6); gelu2(7); h_st(wbB, 1, g == 2); QSTEP(47, tQ);
        SYNC(48, 4);
        QSTEP(48, tO);
#pragma unroll
        for (int j = 0; j < 14; ++j) Hf[j] = lds_ld<bf16x8>(hrow + hf * 16 + j * 32);
#pragma unroll
        for (int i = 0; i < 8; ++i) pq[i] = cvtpk(phi(acc[2 * i]), phi(acc[2 * i + 1]));
    }
    acc = zero16; accP = zero16;

    auto attn_o = [&]() {
        if (g >= 2) {
            const int m = l31, e = 16 * ((m >> 2) & 1) + 4 * (m >> 3) + (m & 3);
            const u32 ka = L0 + L_KVB + (g & 1) * 2560 + e * 80 + hf * 32;
            f32x16 nm = MFMA32(lds_ld<bf16x8>(ka), __builtin_bit_cast(bf16x8, (u32x4){pq[0], pq[1], pq[2], pq[3]}), zero16);
            nm = MFMA32(lds_ld<bf16x8>(ka + 16), __builtin_bit_cast(bf16x8, (u32x4){pq[4], pq[5], pq[6], pq[7]}), nm);
            auto dd = __builtin_amdgcn_permlane32_swap(__float_as_uint(nm[12]), __float_as_uint(nm[12]), false, false);
            const float rd = __builtin_amdgcn_rcpf(__uint_as_float(dd[1]) + 1e-6f);
            const u32 oa = hrow + 448 + (g & 1) * 56 + hf * 32;
#pragma unroll
            for (int i = 0; i < 4; ++i) {
                u32x2 v = {cvtpk(nm[4 * i] * rd, nm[4 * i + 1] * rd), cvtpk(nm[4 * i + 2] * rd, nm[4 * i + 3] * rd)};
                if (i < 3 || hf == 0) lds_st<u32x2>(oa + i * 8, v);
            }
        }
    };
    auto tile_st = [&](const f32x16& a, char* p0, char* p1) {
        st16c(p0, lane * 16, (u32x4){cvtpk(a[0], a[1]), cvtpk(a[2], a[3]), cvtpk(a[4], a[5]), cvtpk(a[6], a[7])});
        st16c(p1, lane * 16, (u32x4){cvtpk(a[8], a[9]), cvtpk(a[10], a[11]), cvtpk(a[12], a[13]), cvtpk(a[14], a[15])});
    };
    auto flag_st = [&](int dest, int j) { if (lane == 0) st8c(wsF + dest * 256 + j * 64 + s * 8, 0u, (u32x2){tag, (u32)(tag64 >> 32)}); };
    char* const otile = wsO + (size_t)(w * 7 * 8 + s) * 1024;
#define OSTEP(SG, K0, ACC) do { \
        if constexpr ((SG) % 2 == 0) { SYNC(SG, 4); \
            if constexpr ((SG) == H2C + 2) attn_o(); \
            if constexpr ((SG) == H2C + 4) { _Pragma("unroll") for (int j = 14; j < 18; ++j) Hf[j] = lds_ld<bf16x8>(hrow + hf * 16 + j * 32); } \
            if constexpr ((SG) == FL01) { flag_st(w, 0); flag_st(w, 1); } } \
        RD((SG) + 1, tO); \
        ACC = MFMA32(Bc0, Hf[K0], ACC); ACC = MFMA32(Bc1, Hf[(K0) + 1], ACC); ROT(); } while (0)
    static_assert(H2C + 4 <= 62 && H2C >= 50 && H2I >= H1C + 4 && H1I >= 18, "schedule");
    OSTEP(49, 0, acc); OSTEP(50, 2, acc); OSTEP(51, 4, acc); OSTEP(52, 6, acc); OSTEP(53, 8, acc); OSTEP(54, 10, acc); OSTEP(55, 12, acc);
    OSTEP(56, 0, accP); OSTEP(57, 2, accP); OSTEP(58, 4, accP); OSTEP(59, 6, accP); OSTEP(60, 8, accP); OSTEP(61, 10, accP); OSTEP(62, 12, accP);
    STAMPR(8);
    OSTEP(63, 14, acc); OSTEP(64, 16, acc);
    OSTEP(65, 14, accP);
    tile_st(acc, otile, otile + 8192); acc = zero16;
    OSTEP(66, 16, accP);
    OSTEP(67, 0, acc);
    tile_st(accP, otile + 2 * 8192, otile + 3 * 8192);
    OSTEP(68, 2, acc); OSTEP(69, 4, acc); OSTEP(70, 6, acc); OSTEP(71, 8, acc); OSTEP(72, 10, acc); OSTEP(73, 12, acc);
    OSTEP(74, 14, acc); OSTEP(75, 16, acc);
    sync_raw<76, (FL01 == 74 ? 2 : 0), 0>(c);
    STAMPR(9);
    tile_st(acc, otile + 4 * 8192, otile + 5 * 8192);

    auto finish_unit = [&](int u) {
        const int j = u >> 1;
        int tk, col;
        if (u < 6) { tk = 32 * (s & 1) + l31; col = 128 * j + 32 * (s >> 1) + 16 * hf + 8 * (u & 1); }
        else { tk = 32 * ((s >> 1) & 1) + l31; col = 384 + 32 * (s >> 2) + 16 * hf + 8 * (s & 1); }
        const size_t eo = (size_t)(64 * t + tk) * DM + col;
        const u32x4 xv = *(const GAS u32x4*)(x + eo);
        const u32x4 bv = *(const GAS u32x4*)(b2 + col);
        const GAS u64* fl = (const GAS u64*)(wsF + s * 256 + j * 64) + (lane & 7);
        for (u32 spin = 0; spin < SPIN_CAP; ++spin) {
            const u64 f = __hip_atomic_load(fl, __ATOMIC_RELAXED, __HIP_MEMORY_SCOPE_AGENT);
            if (__all(f == tag64)) break;
        }
        const char* src = wsO + (size_t)((s * 7 + u) * 8) * 1024;
        u32x4 pv[8];
        const u32 vo = lane * 16;
        asm volatile("global_load_dwordx4 %0, %8, %9 sc1\n\tglobal_load_dwordx4 %1, %8, %9 offset:1024 sc1\n\t"
                     "global_load_dwordx4 %2, %8, %9 offset:2048 sc1\n\tglobal_load_dwordx4 %3, %8, %9 offset:3072 sc1\n\t"
                     "global_load_dwordx4 %4, %8, %10 sc1\n\tglobal_load_dwordx4 %5, %8, %10 offset:1024 sc1\n\t"
                     "global_load_dwordx4 %6, %8, %10 offset:2048 sc1\n\tglobal_load_dwordx4 %7, %8, %10 offset:3072 sc1\n\t"
                     "s_waitcnt vmcnt(0)"
                     : "=&v"(pv[0]), "=&v"(pv[1]), "=&v"(pv[2]), "=&v"(pv[3]), "=&v"(pv[4]), "=&v"(pv[5]), "=&v"(pv[6]), "=&v"(pv[7])
                     : "v"(vo), "s"(src), "s"(src + 4096) : "memory");
        u32x4 ov;
#pragma unroll
        for (int e = 0; e < 4; ++e) {
            float lo = bflo(pv[0][e]), hi = bfhi(pv[0][e]);
#pragma unroll
            for (int i = 1; i < 8; ++i) { lo += bflo(pv[i][e]); hi += bfhi(pv[i][e]); }
            ov[e] = cvtpk(lo + bflo(xv[e]) + bflo(bv[e]), hi + bfhi(xv[e]) + bfhi(bv[e]));
        }
        *(GAS u32x4*)(out + eo) = ov;
    };

    if (g < 2) {
        const u32 tO2 = tO ^ 128u;
        acc = zero16;
#define LSTEP(SG, K0) do { RD(SG, tO2); \
        acc = MFMA32(Bc0, Hf[K0], acc); acc = MFMA32(Bc1, Hf[(K0) + 1], acc); ROT(); \
        if constexpr ((SG) < 80) RD((SG) + 1, tO); \
        acc = MFMA32(Bc0, Hf[(K0) + 2], acc); acc = MFMA32(Bc1, Hf[(K0) + 3], acc); ROT(); } while (0)
        LSTEP(76, 0); LSTEP(77, 4); LSTEP(78, 8);
        RD(80, tO); acc = MFMA32(Bc0, Hf[12], acc); acc = MFMA32(Bc1, Hf[13], acc); ROT();
        LSTEP(80, 14);
        char* o6 = wsO + (size_t)(((2 * w) * 7 + 6) * 8 + s) * 1024;
        tile_st(acc, o6, o6 + 7 * 8192);
        STAMPR(10);
        waitvm<2>(); flag_st(w, 2);
        waitvm<1>(); flag_st(2 * w, 3); flag_st(2 * w + 1, 3);
        STAMPR(11);
    } else {
        waitvm<0>(); flag_st(w, 2);
        STAMPR(10);
        finish_unit(w - 4);
        STAMPR(11);
        if (w < 7) finish_unit(w);
        else if (blockIdx.x == 0 && lane == 0) st8c(ws, 0u, (u32x2){tag, (u32)(tag64 >> 32)});
    }
    STAMPR(12);
    { u64 cy1; asm volatile("s_memtime %0\n\ts_waitcnt lgkmcnt(0)" : "=s"(cy1) :: "memory"); tsv[15] = cy1 - cy0; }
    if (lane == 0) for (int i = 0; i < 16; ++i) ((GAS u64*)(ws + W_TS))[((size_t)blockIdx.x * 8 + w) * 16 + i] = tsv[i];
}

__global__ void k_report(const u64* ts) {
    const int l = threadIdx.x;
    u64 t0 = ~0ull;
    for (int w = l; w < 2048; w += 64) { u64 v = ts[w * 16]; t0 = v < t0 ? v : t0; }
    for (int o = 32; o; o >>= 1) { u64 v = __shfl_xor(t0, o); t0 = v < t0 ? v : t0; }
    float av[13], mx[13];
#pragma unroll
    for (int i = 0; i < 13; ++i) {
        float s = 0.f, m = 0.f;
        for (int w = l; w < 2048; w += 64) { float d = (float)(ts[w * 16 + i] - t0) * 0.01f; s += d; m = d > m ? d : m; }
        for (int o = 32; o; o >>= 1) { s += __shfl_xor(s, o); float v = __shfl_xor(m, o); m = v > m ? v : m; }
        av[i] = s / 2048.f; mx[i] = m;
    }
    float ck = (float)ts[15] / ((float)(ts[12] - ts[0]) * 10.f);
    if (l == 0) printf("ck %.2f | %.1f/%.1f %.1f %.1f %.1f %.1f %.1f %.1f %.1f %.1f %.1f/%.1f %.1f %.1f %.1f/%.1f\n", ck, av[0], mx[0], av[1], av[2], av[3], av[4], av[5], av[6], av[7], av[8], av[9], mx[9], av[10], av[11], av[12], mx[12]);
}
extern "C" void kernel_launch(void* const* d_in, const int* in_sizes, int n_in, void* d_out, int out_size, void* d_ws, size_t ws_size,
                              hipStream_t stream) {
    (void)in_sizes; (void)n_in; (void)out_size; (void)ws_size;
    (void)hipFuncSetAttribute((const void*)fused_block, hipFuncAttributeMaxDynamicSharedMemorySize, LDS_BYTES);
    hipLaunchKernelGGL(fused_block, dim3(256), dim3(512), LDS_BYTES, stream,
                       (const u16*)d_in[0], (const u16*)d_in[1], (const u16*)d_in[2], (const u16*)d_in[4], (const u16*)d_in[5],
                       (const u16*)d_in[6], (const u16*)d_in[7], (u16*)d_out, (char*)d_ws);
    hipLaunchKernelGGL(k_report, dim3(1), dim3(64), 0, stream, (const u64*)((char*)d_ws + W_TS));
}
```

```cpp
#define DIAG
#define HDIAG
#define WHICH 1
#pragma clang diagnostic ignored "-Wint-to-pointer-cast"
#include <hip/hip_runtime.h>
#include <stdint.h>

typedef unsigned short u16;
typedef unsigned u32;
typedef unsigned long long u64;
using bf16x8 = __attribute__((ext_vector_type(8))) short;
using s16x4 = __attribute__((ext_vector_type(4))) short;
using f32x16 = __attribute__((ext_vector_type(16))) float;
using f32x4 = __attribute__((ext_vector_type(4))) float;
using f32x2 = __attribute__((ext_vector_type(2))) float;
using u32x4 = __attribute__((ext_vector_type(4))) unsigned;
using u32x2 = __attribute__((ext_vector_type(2))) unsigned;
#define LAS __attribute__((address_space(3)))
#define GAS __attribute__((address_space(1)))
#define DEV __device__ __forceinline__

#define BSL 4
#define H1I 30
#define H1C 36
#define H2I 48
#define H2C 54
#define FL01 74

namespace {
constexpr int DM = 448, FF = 1792;
constexpr int NSLAB = 81, RING = 12, SLABB = 8192;
constexpr int L_XN = RING * SLABB;
constexpr int XS = 912, HS = 592;
constexpr int L_H = L_XN;
constexpr int L_KVX = L_XN + 64 * HS;
constexpr int L_RED = L_KVX + 16384;
constexpr int L_MISC = L_XN + 64 * XS;
constexpr int L_R2 = L_MISC;
constexpr int L_B1 = L_MISC + 256;
constexpr int L_KVB = L_B1 + 1024;
constexpr int L_DUMMY = L_KVB + 5120;
constexpr int LDS_BYTES = L_DUMMY + 256;
static_assert(L_RED + 2048 <= L_MISC, "lds");
static_assert(LDS_BYTES <= 163840, "lds");
constexpr size_t W_E = 0, W_FL = 4096, W_P = 0x20000, W_R = W_P + (4u << 20), W_O = W_R + 65536;
constexpr size_t W_TS = 32u << 20;
constexpr u32 SPIN_CAP = 1u << 18;

typedef __bf16 bf16x2_t __attribute__((ext_vector_type(2)));
DEV u32 cvtpk(float lo, float hi) { f32x2 v = {lo, hi}; return __builtin_bit_cast(u32, __builtin_convertvector(v, bf16x2_t)); }
DEV float bflo(u32 v) { return __uint_as_float(v << 16); }
DEV float bfhi(u32 v) { return __uint_as_float(v & 0xffff0000u); }
template <int CTRL> DEV float dpp(float v) { return __int_as_float(__builtin_amdgcn_update_dpp(0, __float_as_int(v), CTRL, 0xf, 0xf, true)); }
template <int IMM> DEV void glds16(const char* sb, u32 voff, u32 ldsw) {
    asm volatile("s_add_u32 m0, %2, %3\n\ts_nop 0\n\tglobal_load_lds_dwordx4 %0, %1" :: "v"(voff), "s"(sb), "s"(ldsw), "n"(IMM) : "memory", "scc");
}
template <int IMM> DEV void glds16c(const char* sb, u32 voff, u32 ldsw) {
    asm volatile("s_add_u32 m0, %2, %3\n\ts_nop 0\n\tglobal_load_lds_dwordx4 %0, %1 sc1" :: "v"(voff), "s"(sb), "s"(ldsw), "n"(IMM) : "memory", "scc");
}
DEV void touch(const char* sb, u32 voff, u32 lds) {
    asm volatile("s_mov_b32 m0, %2\n\ts_nop 0\n\tglobal_load_lds_dword %0, %1" :: "v"(voff), "s"(sb), "s"(lds) : "memory");
}
DEV void st16c(char* sb, u32 voff, u32x4 v) { asm volatile("global_store_dwordx4 %0, %1, %2 sc1\n\ts_nop 1" :: "v"(voff), "v"(v), "s"(sb) : "memory"); }
DEV void st8c(char* sb, u32 voff, u32x2 v) { asm volatile("global_store_dwordx2 %0, %1, %2 sc1" :: "v"(voff), "v"(v), "s"(sb) : "memory"); }
template <int N> DEV void waitvm() { asm volatile("s_waitcnt vmcnt(%0)" :: "n"(N) : "memory"); }
template <typename T> DEV T lds_ld(u32 a) { return *(const LAS T*)(a); }
template <typename T> DEV void lds_st(u32 a, T v) { *(LAS T*)(a) = v; }
typedef short v4i16_t __attribute__((ext_vector_type(4)));
DEV bf16x8 trfrag(u32 a) {
    s16x4 lo = __builtin_amdgcn_ds_read_tr16_b64_v4i16((LAS v4i16_t*)(a));
    s16x4 hi = __builtin_amdgcn_ds_read_tr16_b64_v4i16((LAS v4i16_t*)(a + 1024));
    return __builtin_shufflevector(lo, hi, 0, 1, 2, 3, 4, 5, 6, 7);
}
#define MFMA32(a, b, c) __builtin_amdgcn_mfma_f32_32x32x16_bf16(a, b, c, 0, 0, 0)
#define MFMA16(a, b, c) __builtin_amdgcn_mfma_f32_16x16x32_bf16(a, b, c, 0, 0, 0)
#define SBAR() __builtin_amdgcn_sched_barrier(0)

DEV float phi(float t) { float e = __builtin_amdgcn_exp2f(t * 1.4426950408889634f); return (t > 0.f ? t + 1.f : e) + 1e-6f; }
DEV float gelu(float u) {
    const float k0 = -2.f * 0.7978845608028654f * 1.4426950408889634f, k1 = k0 * 0.044715f;
    float p = __builtin_fmaf(u * u, k1, k0);
    float e = __builtin_amdgcn_exp2f(u * p);
    return u * __builtin_amdgcn_rcpf(1.f + e);
}

constexpr int nglds(int s) { return s < 2 ? 0 : ((s + 10 < NSLAB) + (s + 11 < NSLAB)); }
constexpr int extra(int s) { return (s == 16 ? 2 : 0) + (s == H1I ? 2 : 0) + (s == H2I ? 1 : 0) + (s == 64 ? 2 : 0) + (s == 66 ? 2 : 0) + (s == FL01 ? 2 : 0); }
constexpr int younger_than_extra(int from, int upto) { int n = 0; for (int i = from + 2; i < upto; i += 2) n += nglds(i) + extra(i); return n; }
constexpr int vmwait(int s) {
    if (s + 3 <= 11) { int n = 11 - (s + 3); for (int i = 0; i < s; i += 2) n += nglds(i) + extra(i); return n; }
    int last = (s + 3 < NSLAB) ? s - 8 : 70;
    return extra(last) + younger_than_extra(last, s);
}

struct Ctx {
    const char *gKV, *gW1a, *gW1b, *gQ, *gW2, *gWo;
    u32 vKV, vQ, vW1, vW2, vWo1, vL, vLd, vLo;
    u32 ldsw;
};
template <int SG> DEV void issue(const Ctx& c) {
    constexpr int IMM = (SG % RING) * SLABB;
    if constexpr (SG < 14) glds16<IMM>(c.gKV + SG * 32 * 2688, c.vKV, c.ldsw);
    else if constexpr (SG < 28) glds16<IMM>(c.gW1a + (SG - 14) * 32 * 3584, c.vW1, c.ldsw);
    else if constexpr (SG < 42) glds16<IMM>(c.gW1b + (SG - 28) * 32 * 3584, c.vW1, c.ldsw);
    else if constexpr (SG < 49) glds16<IMM>(c.gQ + (SG - 42) * 32 * 2688, c.vQ, c.ldsw);
    else if constexpr (SG < 56) glds16<IMM>(c.gW2 + (SG - 49) * 32 * 896, c.vW2, c.ldsw);
    else if constexpr (SG < 63) glds16<IMM>(c.gW2 + (SG - 56) * 32 * 896 + 256, c.vW2, c.ldsw);
    else if constexpr (SG == 63) glds16<IMM>(c.gWo, c.vW2, c.ldsw);
    else if constexpr (SG == 64) glds16<IMM>(c.gWo, c.vWo1, c.ldsw);
    else if constexpr (SG == 65) glds16<IMM>(c.gWo + 256, c.vW2, c.ldsw);
    else if constexpr (SG == 66) glds16<IMM>(c.gWo + 256, c.vWo1, c.ldsw);
    else if constexpr (SG < 74) glds16<IMM>(c.gW2 + (SG - 67) * 32 * 896 + 512, c.vW2, c.ldsw);
    else if constexpr (SG == 74) glds16<IMM>(c.gWo + 512, c.vW2, c.ldsw);
    else if constexpr (SG == 75) glds16<IMM>(c.gWo + 512, c.vWo1, c.ldsw);
    else if constexpr (SG < 79) glds16<IMM>(c.gW2 + (SG - 76) * 64 * 896 + 768, c.vL, c.ldsw);
    else if constexpr (SG == 79) glds16<IMM>(c.gW2 + 192 * 896 + 768, c.vLd, c.ldsw);
    else glds16<IMM>(c.gWo + 768, c.vLo, c.ldsw);
}
template <int SG, int VM, int LG> DEV void sync_raw(const Ctx& c) {
    SBAR();
    asm volatile("s_waitcnt vmcnt(%0) lgkmcnt(%1)\n\ts_barrier" :: "n"(VM), "n"(LG) : "memory");
    if constexpr (SG >= 2 && SG + 10 < NSLAB) issue<SG + 10>(c);
    if constexpr (SG >= 2 && SG + 11 < NSLAB) issue<SG + 11>(c);
    SBAR();
}
constexpr u32 slot_off(int sg) { return (u32)((sg % RING) * SLABB); }
}

#define STAMP(i) do { if (lane == 0) { SBAR(); ((GAS u64*)(ws + W_TS))[((size_t)blockIdx.x * 8 + w) * 16 + (i)] = __builtin_amdgcn_s_memrealtime(); SBAR(); } } while (0)
#define STAMPR(i) do { SBAR(); asm volatile("s_memrealtime %0\n\ts_waitcnt lgkmcnt(0)" : "=s"(tsv[i]) :: "memory"); SBAR(); } while (0)

__global__ __launch_bounds__(512, 2) void fused_block(const u16* __restrict__ x, const u16* __restrict__ wqkv, const u16* __restrict__ wo,
                                                      const u16* __restrict__ w1, const u16* __restrict__ b1, const u16* __restrict__ w2,
                                                      const u16* __restrict__ b2, u16* __restrict__ out, char* __restrict__ ws) {
    constexpr u32 L0 = 0;
    asm volatile("" :: "s"(x), "s"(wqkv), "s"(wo), "s"(w1), "s"(b1), "s"(w2), "s"(b2), "s"(out), "s"(ws));
    const int lane = threadIdx.x & 63;
    const int w = __builtin_amdgcn_readfirstlane(threadIdx.x >> 6);
    const int th = w & 1, g = w >> 1;
    const int xcd = blockIdx.x & 7, jj = blockIdx.x >> 3;
    const int s = (xcd % (8 / BSL)) * BSL + (jj % BSL);
    const int t = (xcd / (8 / BSL)) * (32 / BSL) + jj / BSL;
    u64 tsv[16];
    for (int i = 0; i < 16; ++i) tsv[i] = 0;
    STAMPR(0);
    u64 cy0; asm volatile("s_memtime %0\n\ts_waitcnt lgkmcnt(0)" : "=s"(cy0) :: "memory");

    u32x4 xr[7];
    const int xrow = 8 * w + (lane >> 3);
    const int rot = (jj % BSL) * (8 / BSL);
    {
        const char* xb = (const char*)x + (size_t)t * (64 * 896);
        const u32 vx = xrow * 896 + (lane & 7) * 16;
#pragma unroll
        for (int j = 0; j < 7; ++j) {
            int jr = j + rot; jr = jr >= 7 ? jr - 7 : jr;
            asm volatile("global_load_dwordx4 %0, %1, %2" : "=v"(xr[j]) : "v"(vx), "s"(xb + jr * 128));
        }
    }
    Ctx c;
    {
        const int r = lane >> 4, ch = (lane & 15) ^ (r << 2), seg = ch >> 3, c8 = ch & 7, row = 4 * w + r;
        const int c7 = c8 > 6 ? 6 : c8;
        c.vKV = row * 2688 + seg * 896 + c7 * 16;
        c.vQ = (row + seg * 224) * 2688 + c7 * 16;
        c.vW1 = row * 3584 + ch * 16;
        c.vW2 = row * 896 + ch * 16;
        c.vWo1 = (row + 32 > 55 ? 55 : row + 32) * 896 + ch * 16;
        c.vL = (row + 32 * seg) * 896 + c8 * 16;
        c.vLd = row * 896 + c8 * 16;
        c.vLo = (row + 32 * seg > 55 ? 55 : row + 32 * seg) * 896 + c8 * 16;
        c.gKV = (const char*)wqkv + 896 + 112 * s;
        c.gQ = (const char*)wqkv + 112 * s;
        const int wb = 448 * s - 64 * (s & 1);
        c.gW1a = (const char*)w1 + wb + ((s & 1) ? 256 : 0);
        c.gW1b = (const char*)w1 + wb + ((s & 1) ? 0 : 256);
        c.gW2 = (const char*)w2 + (size_t)s * (224 * 896);
        c.gWo = (const char*)wo + (size_t)s * (56 * 896);
        c.ldsw = L0 + w * 1024;
    }
    constexpr int NPF = 2;
    {
        const int s0 = s - (jj % BSL);
        const int tid = threadIdx.x;
        {
            constexpr int LPS = (BSL == 1) ? 2 : (BSL == 2 ? 3 : 4);
            const int base = (112 * s0) & ~127;
            int i = tid; const int n = 14 * 3 * LPS;
            u32 off;
            const char* sb = (const char*)wqkv;
            i = i < n ? i : n - 1;
            const int m = i / (14 * LPS), rem = i % (14 * LPS), rr = rem / LPS, ll = rem % LPS;
            const int mm = m == 2 ? 0 : m + 1;
            int col = base + ll * 128; col = col > 768 ? 768 : col;
            off = (14 * jj + rr) * 2688 + mm * 896 + col;
            if (w * 64 < n) touch(sb, off, L0 + L_DUMMY);
            else {
                constexpr int LW = (BSL == 1) ? 4 : (7 * BSL / 2);
                constexpr int W0 = (14 * 3 * LPS + 63) / 64;
                int k = tid - W0 * 64; const int n1 = 14 * LW;
                k = k < n1 ? k : n1 - 1;
                const int bw = (448 * s0) & ~127;
                touch((const char*)w1, (14 * jj + k / LW) * 3584 + bw + (k % LW) * 128, L0 + L_DUMMY);
            }
        }
        {
            constexpr int N2 = 49 * BSL, NO = (392 * BSL + 31) / 32;
            if (w == 7) {
                const int wb = 448 * s - 64 * (s & 1);
                glds16<L_B1>((const char*)b1 + wb, (lane & 31) * 16, L0);
            } else if (w >= 5) {
                int k = jj * NO + (tid - 320 < NO ? tid - 320 : NO - 1); k = k < 392 * BSL ? k : 392 * BSL - 1;
                touch((const char*)wo + (size_t)s0 * (56 * 896), k * 128, L0 + L_DUMMY);
            } else {
                int k = tid < N2 ? tid : N2 - 1;
                touch((const char*)w2 + (size_t)s0 * (224 * 896), (jj * N2 + k) * 128, L0 + L_DUMMY);
            }
        }
    }
    issue<0>(c); issue<1>(c); issue<2>(c); issue<3>(c); issue<4>(c); issue<5>(c);
    issue<6>(c); issue<7>(c); issue<8>(c); issue<9>(c); issue<10>(c); issue<11>(c);
    u64 epoch;
    asm volatile("s_load_dwordx2 %0, %1, 0x0" : "=s"(epoch) : "s"(ws));

    asm volatile("s_waitcnt vmcnt(%8) lgkmcnt(0)" : "+v"(xr[0]), "+v"(xr[1]), "+v"(xr[2]), "+v"(xr[3]), "+v"(xr[4]), "+v"(xr[5]), "+v"(xr[6]), "+s"(epoch) : "n"(12 + NPF));
    STAMPR(1);
    const u64 tag64 = epoch + 0x100000001ull;
    const u32 tag = (u32)tag64;
    {
        float s1 = 0.f, s2 = 0.f, s1b = 0.f, s2b = 0.f;
#pragma unroll
        for (int j = 0; j < 7; ++j)
#pragma unroll
            for (int e = 0; e < 4; ++e) {
                float a = bflo(xr[j][e]), b = bfhi(xr[j][e]);
                s1 += a; s2 = __builtin_fmaf(a, a, s2); s1b += b; s2b = __builtin_fmaf(b, b, s2b);
            }
        s1 += s1b; s2 += s2b;
        s1 += dpp<0xB1>(s1); s2 += dpp<0xB1>(s2);
        s1 += dpp<0x4E>(s1); s2 += dpp<0x4E>(s2);
        s1 += dpp<0x141>(s1); s2 += dpp<0x141>(s2);
        const float mean = s1 * (1.f / DM);
        const float var = __builtin_fmaxf(__builtin_fmaf(-mean, mean, s2 * (1.f / DM)), 0.f);
        const float rstd = __builtin_amdgcn_rsqf(var + 1e-5f);
        const float nb = -mean * rstd;
        const u32 xa = L0 + L_XN + xrow * XS + (lane & 7) * 16;
#pragma unroll
        for (int j = 0; j < 7; ++j) {
            int jr = j + rot; jr = jr >= 7 ? jr - 7 : jr;
            u32x4 o;
#pragma unroll
            for (int e = 0; e < 4; ++e) o[e] = cvtpk(__builtin_fmaf(bflo(xr[j][e]), rstd, nb), __builtin_fmaf(bfhi(xr[j][e]), rstd, nb));
            lds_st<u32x4>(xa + jr * 128, o);
        }
        if ((lane & 7) == 0) lds_st<float>(L0 + L_R2 + xrow * 4, __builtin_amdgcn_rsqf(var * rstd * rstd + 1e-5f));
    }
    asm volatile("s_waitcnt lgkmcnt(0)\n\ts_barrier" ::: "memory");
    const int l31 = lane & 31, hf = lane >> 5;
    bf16x8 A[28];
    {
        const u32 a = L0 + L_XN + (32 * th + l31) * XS + hf * 16;
#pragma unroll
        for (int j = 0; j < 28; ++j) A[j] = lds_ld<bf16x8>(a + j * 32);
    }
    const float r2 = lds_ld<float>(L0 + L_R2 + (32 * th + l31) * 4);

    const int blk = (lane >> 4) & 1, q = (lane & 15) >> 2, p = lane & 3;
    const u32 trrow = L0 + (8 * hf + q) * 256;
    const u32 mstd = 32 * blk + 8 * p, mperm = 32 * (p & 1) + 16 * blk + 8 * (p >> 1);
    const u32 tKV = trrow + ((((g >> 1) * 128 + (g & 1) * 56) + mstd) ^ (q << 6));
    const int gq = (s & 1) ? 3 - g : g;
    const u32 tW1 = trrow + ((64 * gq + mperm) ^ (q << 6));
    const u32 tQ = trrow + (((g & 1) * 56 + mperm) ^ (q << 6));
    const u32 tO = trrow + ((64 * g + mperm) ^ (q << 6));
    const u32 hrow = L0 + L_H + (32 * th + l31) * HS;

    char* const wsP = ws + W_P + (size_t)s * (32 * 32 * 512);
    char* const wsR = ws + W_R + (size_t)s * 8192;
    char* const wsO = ws + W_O + (size_t)t * (8 * 7 * 8 * 1024);
    char* const wsF = ws + W_FL + (size_t)t * (8 * 256);

    bf16x8 Bc0, Bc1, Bn0, Bn1;
    f32x16 acc, accP;
    const f32x16 zero16 = {0.f, 0.f, 0.f, 0.f, 0.f, 0.f, 0.f, 0.f, 0.f, 0.f, 0.f, 0.f, 0.f, 0.f, 0.f, 0.f};

    auto hop1_issue = [&]() { const char* src = wsP + (size_t)t * 16384 + w * 2048; glds16c<L_KVX>(src, lane * 16, L0 + w * 2048); glds16c<L_KVX + 1024>(src + 1024, lane * 16, L0 + w * 2048); };
    auto hop1_check = [&]() {
        u32x4 a, b;
        for (u32 spin = 0;; ++spin) {
            a = lds_ld<u32x4>(L0 + L_KVX + w * 2048 + lane * 16); b = lds_ld<u32x4>(L0 + L_KVX + w * 2048 + 1024 + lane * 16);
            const bool ok = a[1] == tag && a[3] == tag && b[1] == tag && b[3] == tag;
            if (__all(ok) || spin >= SPIN_CAP) break;
            hop1_issue(); waitvm<0>();
        }
        float v0 = __uint_as_float(a[0]) + __uint_as_float(b[0]), v1 = __uint_as_float(a[2]) + __uint_as_float(b[2]);
        auto r0 = __builtin_amdgcn_permlane32_swap(__float_as_uint(v0), __float_as_uint(v0), false, false);
        auto r1 = __builtin_amdgcn_permlane32_swap(__float_as_uint(v1), __float_as_uint(v1), false, false);
        f32x2 o = {__uint_as_float(r0[0]) + __uint_as_float(r0[1]), __uint_as_float(r1[0]) + __uint_as_float(r1[1])};
        if (lane < 32) lds_st<f32x2>(L0 + L_RED + w * 256 + lane * 8, o);
    };
    auto hop1_publish = [&]() {
        if (w == 6) {
            f32x2 v = lds_ld<f32x2>(L0 + L_RED + l31 * 8);
#pragma unroll
            for (int i = 1; i < 8; ++i) v += lds_ld<f32x2>(L0 + L_RED + i * 256 + l31 * 8);
            if (lane < 32) st8c(wsR + t * 256, lane * 8, (u32x2){cvtpk(v[0], v[1]), tag});
        }
    };
    auto hop2_issue = [&]() { glds16c<L_KVX>(wsR + w * 1024, lane * 16, L0 + w * 2048); };
    auto hop2_check = [&]() {
        u32x4 a;
        for (u32 spin = 0;; ++spin) {
            a = lds_ld<u32x4>(L0 + L_KVX + w * 2048 + lane * 16);
            const bool ok = a[1] == tag && a[3] == tag;
            if (__all(ok) || spin >= SPIN_CAP) break;
            hop2_issue(); waitvm<0>();
        }
        const int Ln = 32 * ((lane >> 4) & 1) + 2 * (lane & 15);
        const u32 ad = L0 + L_KVB + (w >> 2) * 2560 + (16 * (w & 1) + (Ln & 15)) * 80 + (16 * ((w >> 1) & 1) + 4 * (Ln >> 4) + 2 * hf) * 2;
        lds_st<u32>(ad, a[0]); lds_st<u32>(ad + 80, a[2]);
    };
#define SYNC(SG, LG) do { \
        constexpr bool full_ = ((SG) == 16 || (SG) == H1C + 2 || (SG) == 48 || (SG) == H2C + 2 || (SG) == H2C + 4); \
        constexpr int vmr_ = vmwait(SG); \
        constexpr int vm1_ = (SG) == H1C ? younger_than_extra(H1I, SG) : ((SG) == H2C ? younger_than_extra(H2I, SG) : ((SG) == FL01 ? younger_than_extra(66, SG) : 63)); \
        sync_raw<SG, (vm1_ < vmr_ ? vm1_ : vmr_), (full_ ? 0 : (LG))>(c); \
        if constexpr ((SG) == H1I) hop1_issue(); \
        if constexpr ((SG) == H2I) hop2_issue(); \
        if constexpr ((SG) == H1C) { hop1_check(); STAMPR(5); } \
        if constexpr ((SG) == H1C + 2) hop1_publish(); \
        if constexpr ((SG) == H2C) { hop2_check(); STAMPR(7); } \
    } while (0)
#define RD(SG, T) do { Bn0 = trfrag((T) + slot_off(SG)); Bn1 = trfrag((T) + slot_off(SG) + 4096); } while (0)
#define ROT() do { Bc0 = Bn0; Bc1 = Bn1; } while (0)

    u32 hp[8];
    u32x4 bq0, bq1;
    auto gelu2 = [&](int i) {
        const u32 bb = i < 4 ? bq0[i & 3] : bq1[i & 3];
        hp[i] = cvtpk(gelu(__builtin_fmaf(accP[2 * i], r2, bflo(bb))), gelu(__builtin_fmaf(accP[2 * i + 1], r2, bfhi(bb))));
    };
    auto bias_ld = [&](int wblk) { const u32 a = L0 + L_B1 + wblk * 64 + hf * 32; bq0 = lds_ld<u32x4>(a); bq1 = lds_ld<u32x4>(a + 16); };
    auto h_st = [&](int wblk, int half, bool live) {
        const u32 a = live ? hrow + (wblk - (s & 1)) * 64 + hf * 32 + half * 16 : L0 + L_DUMMY;
        lds_st<u32x4>(a, (u32x4){hp[4 * half], hp[4 * half + 1], hp[4 * half + 2], hp[4 * half + 3]});
    };

    SYNC(0, 0);
    STAMPR(2);
    RD(0, tKV); ROT();
    acc = zero16;
#define G1STEP(SG) do { if constexpr ((SG) % 2 == 0 && (SG) > 0) SYNC(SG, 4); \
        if constexpr ((SG) < 13) RD((SG) + 1, tKV); else RD((SG) + 1, tW1); \
        acc = MFMA32(A[2 * (SG)], Bc0, acc); acc = MFMA32(A[2 * (SG) + 1], Bc1, acc); ROT(); } while (0)
    G1STEP(0); G1STEP(1); G1STEP(2); G1STEP(3); G1STEP(4); G1STEP(5); G1STEP(6);
    G1STEP(7); G1STEP(8); G1STEP(9); G1STEP(10); G1STEP(11); G1STEP(12); G1STEP(13);
    accP = acc; acc = zero16;
    STAMPR(3);

#define W1STEP(SG, B0, TN) do { RD((SG) + 1, TN); \
        acc = MFMA32(Bc0, A[2 * ((SG) - (B0))], acc); acc = MFMA32(Bc1, A[2 * ((SG) - (B0)) + 1], acc); ROT(); } while (0)
    SYNC(14, 4);
    W1STEP(14, 14, tW1);
    {
        u32 pk[8];
        if (g < 2) {
#pragma unroll
            for (int i = 0; i < 8; ++i) { u32 v = cvtpk(phi(accP[2 * i]), phi(accP[2 * i + 1])); pk[i] = l31 < 28 ? v : 0u; }
        } else {
#pragma unroll
            for (int i = 0; i < 8; ++i) { u32 v = cvtpk(accP[2 * i], accP[2 * i + 1]); pk[i] = l31 == 28 ? 0x3f803f80u : v; }
        }
        const u32 a = L0 + L_KVX + (g * 8 + th * 4 + hf * 2) * 512 + l31 * 16;
        lds_st<u32x4>(a, (u32x4){pk[0], pk[1], pk[2], pk[3]});
        lds_st<u32x4>(a + 512, (u32x4){pk[4], pk[5], pk[6], pk[7]});
        if (w == 7) lds_st<u32x4>(L0 + L_H + lane * HS + 560, (u32x4){0u, 0u, 0u, 0u});
    }
    W1STEP(15, 14, tW1);
    SYNC(16, 4);
    {
        const u32 ka = L0 + L_KVX + ((w >> 2) * 8 + (lane >> 4)) * 512 + (16 * ((w >> 1) & 1) + (lane & 15)) * 16;
        const u32 va = L0 + L_KVX + ((2 + (w >> 2)) * 8 + (lane >> 4)) * 512 + (16 * (w & 1) + (lane & 15)) * 16;
        f32x4 kv = {0.f, 0.f, 0.f, 0.f};
        kv = MFMA16(lds_ld<bf16x8>(ka), lds_ld<bf16x8>(va), kv);
        kv = MFMA16(lds_ld<bf16x8>(ka + 2048), lds_ld<bf16x8>(va + 2048), kv);
        const u32 vo = ((4 * w + hf) * 32 + t) * 512 + l31 * 16;
        st16c(wsP, vo, (u32x4){__float_as_uint(kv[0]), tag, __float_as_uint(kv[1]), tag});
        st16c(wsP + 2 * 16384, vo, (u32x4){__float_as_uint(kv[2]), tag, __float_as_uint(kv[3]), tag});
    }
    STAMPR(4);
    W1STEP(16, 14, tW1); W1STEP(17, 14, tW1);
#define W1PAIR(SG, B0) do { SYNC(SG, 4); W1STEP(SG, B0, tW1); W1STEP((SG) + 1, B0, tW1); } while (0)
    W1PAIR(18, 14); W1PAIR(20, 14); W1PAIR(22, 14); W1PAIR(24, 14); W1PAIR(26, 14);
    accP = acc; acc = zero16;

    const int wbA = (s & 1) ? 7 - g : g, wbB = (s & 1) ? 3 - g : 4 + g;
    SYNC(28, 4); bias_ld(wbA); W1STEP(28, 28, tW1); W1STEP(29, 28, tW1); gelu2(0);
    SYNC(30, 4); W1STEP(30, 28, tW1); gelu2(1); W1STEP(31, 28, tW1);
    SYNC(32, 4); W1STEP(32, 28, tW1); gelu2(2); W1STEP(33, 28, tW1); gelu2(3);
    SYNC(34, 4); h_st(wbA, 0, true); W1STEP(34, 28, tW1); gelu2(4); W1STEP(35, 28, tW1);
    SYNC(36, 4); W1STEP(36, 28, tW1); gelu2(5); W1STEP(37, 28, tW1);
    SYNC(38, 4); W1STEP(38, 28, tW1); gelu2(6); W1STEP(39, 28, tW1);
    SYNC(40, 4); W1STEP(40, 28, tW1); gelu2(7); h_st(wbA, 1, true); W1STEP(41, 28, tQ);
    accP = acc; acc = zero16;
    STAMPR(6);

    bf16x8 Hf[18];
    u32 pq[8];
    if (g < 2) {
        SYNC(42, 4); bias_ld(wbB); gelu2(0); gelu2(1); gelu2(2);
        SYNC(44, 4); gelu2(3); h_st(wbB, 0, true); gelu2(4); gelu2(5);
        SYNC(46, 4); gelu2(6); gelu2(7); h_st(wbB, 1, true);
        SYNC(48, 4);
        RD(49, tO); ROT();
#pragma unroll
        for (int j = 0; j < 14; ++j) Hf[j] = lds_ld<bf16x8>(hrow + hf * 16 + j * 32);
#pragma unroll
        for (int i = 0; i < 8; ++i) pq[i] = 0;
    } else {
        const u32 tQ2 = tQ ^ 128u;
#define QSTEP(SG, TN) do { RD(SG, tQ2); \
        acc = MFMA32(Bc0, A[2 * ((SG) - 42)], acc); acc = MFMA32(Bc1, A[2 * ((SG) - 42) + 1], acc); ROT(); \
        RD((SG) + 1, TN); \
        acc = MFMA32(Bc0, A[14 + 2 * ((SG) - 42)], acc); acc = MFMA32(Bc1, A[15 + 2 * ((SG) - 42)], acc); ROT(); } while (0)
        SYNC(42, 4); bias_ld(wbB); QSTEP(42, tQ); gelu2(0); QSTEP(43, tQ); gelu2(1); gelu2(2);
        SYNC(44, 4); QSTEP(44, tQ); gelu2(3); h_st(wbB, 0, g == 2); gelu2(4); QSTEP(45, tQ); gelu2(5);
        SYNC(46, 4); QSTEP(46, tQ); gelu2(6); gelu2(7); h_st(wbB, 1, g == 2); QSTEP(47, tQ);
        SYNC(48, 4);
        QSTEP(48, tO);
#pragma unroll
        for (int j = 0; j < 14; ++j) Hf[j] = lds_ld<bf16x8>(hrow + hf * 16 + j * 32);
#pragma unroll
        for (int i = 0; i < 8; ++i) pq[i] = cvtpk(phi(acc[2 * i]), phi(acc[2 * i + 1]));
    }
    acc = zero16; accP = zero16;

    auto attn_o = [&]() {
        if (g >= 2) {
            const int m = l31, e = 16 * ((m >> 2) & 1) + 4 * (m >> 3) + (m & 3);
            const u32 ka = L0 + L_KVB + (g & 1) * 2560 + e * 80 + hf * 32;
            f32x16 nm = MFMA32(lds_ld<bf16x8>(ka), __builtin_bit_cast(bf16x8, (u32x4){pq[0], pq[1], pq[2], pq[3]}), zero16);
            nm = MFMA32(lds_ld<bf16x8>(ka + 16), __builtin_bit_cast(bf16x8, (u32x4){pq[4], pq[5], pq[6], pq[7]}), nm);
            auto dd = __builtin_amdgcn_permlane32_swap(__float_as_uint(nm[12]), __float_as_uint(nm[12]), false, false);
            const float rd = __builtin_amdgcn_rcpf(__uint_as_float(dd[1]) + 1e-6f);
            const u32 oa = hrow + 448 + (g & 1) * 56 + hf * 32;
#pragma unroll
            for (int i = 0; i < 4; ++i) {
                u32x2 v = {cvtpk(nm[4 * i] * rd, nm[4 * i + 1] * rd), cvtpk(nm[4 * i + 2] * rd, nm[4 * i + 3] * rd)};
                if (i < 3 || hf == 0) lds_st<u32x2>(oa + i * 8, v);
            }
        }
    };
    auto tile_st = [&](const f32x16& a, char* p0, char* p1) {
        st16c(p0, lane * 16, (u32x4){cvtpk(a[0], a[1]), cvtpk(a[2], a[3]), cvtpk(a[4], a[5]), cvtpk(a[6], a[7])});
        st16c(p1, lane * 16, (u32x4){cvtpk(a[8], a[9]), cvtpk(a[10], a[11]), cvtpk(a[12], a[13]), cvtpk(a[14], a[15])});
    };
    auto flag_st = [&](int dest, int j) { if (lane == 0) st8c(wsF + dest * 256 + j * 64 + s * 8, 0u, (u32x2){tag, (u32)(tag64 >> 32)}); };
    char* const otile = wsO + (size_t)(w * 7 * 8 + s) * 1024;
#define OSTEP(SG, K0, ACC) do { \
        if constexpr ((SG) % 2 == 0) { SYNC(SG, 4); \
            if constexpr ((SG) == H2C + 2) attn_o(); \
            if constexpr ((SG) == H2C + 4) { _Pragma("unroll") for (int j = 14; j < 18; ++j) Hf[j] = lds_ld<bf16x8>(hrow + hf * 16 + j * 32); } \
            if constexpr ((SG) == FL01) { flag_st(w, 0); flag_st(w, 1); } } \
        RD((SG) + 1, tO); \
        ACC = MFMA32(Bc0, Hf[K0], ACC); ACC = MFMA32(Bc1, Hf[(K0) + 1], ACC); ROT(); } while (0)
    static_assert(H2C + 4 <= 62 && H2C >= 50 && H2I >= H1C + 4 && H1I >= 18, "schedule");
    OSTEP(49, 0, acc); OSTEP(50, 2, acc); OSTEP(51, 4, acc); OSTEP(52, 6, acc); OSTEP(53, 8, acc); OSTEP(54, 10, acc); OSTEP(55, 12, acc);
    OSTEP(56, 0, accP); OSTEP(57, 2, accP); OSTEP(58, 4, accP); OSTEP(59, 6, accP); OSTEP(60, 8, accP); OSTEP(61, 10, accP); OSTEP(62, 12, accP);
    STAMPR(8);
    OSTEP(63, 14, acc); OSTEP(64, 16, acc);
    OSTEP(65, 14, accP);
    tile_st(acc, otile, otile + 8192); acc = zero16;
    OSTEP(66, 16, accP);
    OSTEP(67, 0, acc);
    tile_st(accP, otile + 2 * 8192, otile + 3 * 8192);
    OSTEP(68, 2, acc); OSTEP(69, 4, acc); OSTEP(70, 6, acc); OSTEP(71, 8, acc); OSTEP(72, 10, acc); OSTEP(73, 12, acc);
    OSTEP(74, 14, acc); OSTEP(75, 16, acc);
    sync_raw<76, (FL01 == 74 ? 2 : 0), 0>(c);
    STAMPR(9);
    tile_st(acc, otile + 4 * 8192, otile + 5 * 8192);

    auto finish_unit = [&](int u) {
        const int j = u >> 1;
        int tk, col;
        if (u < 6) { tk = 32 * (s & 1) + l31; col = 128 * j + 32 * (s >> 1) + 16 * hf + 8 * (u & 1); }
        else { tk = 32 * ((s >> 1) & 1) + l31; col = 384 + 32 * (s >> 2) + 16 * hf + 8 * (s & 1); }
        const size_t eo = (size_t)(64 * t + tk) * DM + col;
        const u32x4 xv = *(const GAS u32x4*)(x + eo);
        const u32x4 bv = *(const GAS u32x4*)(b2 + col);
        const GAS u64* fl = (const GAS u64*)(wsF + s * 256 + j * 64) + (lane & 7);
        for (u32 spin = 0; spin < SPIN_CAP; ++spin) {
            const u64 f = __hip_atomic_load(fl, __ATOMIC_RELAXED, __HIP_MEMORY_SCOPE_AGENT);
            if (__all(f == tag64)) break;
        }
        const char* src = wsO + (size_t)((s * 7 + u) * 8) * 1024;
        u32x4 pv[8];
        const u32 vo = lane * 16;
        asm volatile("global_load_dwordx4 %0, %8, %9 sc1\n\tglobal_load_dwordx4 %1, %8, %9 offset:1024 sc1\n\t"
                     "global_load_dwordx4 %2, %8, %9 offset:2048 sc1\n\tglobal_load_dwordx4 %3, %8, %9 offset:3072 sc1\n\t"
                     "global_load_dwordx4 %4, %8, %10 sc1\n\tglobal_load_dwordx4 %5, %8, %10 offset:1024 sc1\n\t"
                     "global_load_dwordx4 %6, %8, %10 offset:2048 sc1\n\tglobal_load_dwordx4 %7, %8, %10 offset:3072 sc1\n\t"
                     "s_waitcnt vmcnt(0)"
                     : "=&v"(pv[0]), "=&v"(pv[1]), "=&v"(pv[2]), "=&v"(pv[3]), "=&v"(pv[4]), "=&v"(pv[5]), "=&v"(pv[6]), "=&v"(pv[7])
                     : "v"(vo), "s"(src), "s"(src + 4096) : "memory");
        u32x4 ov;
#pragma unroll
        for (int e = 0; e < 4; ++e) {
            float lo = bflo(pv[0][e]), hi = bfhi(pv[0][e]);
#pragma unroll
            for (int i = 1; i < 8; ++i) { lo += bflo(pv[i][e]); hi += bfhi(pv[i][e]); }
            ov[e] = cvtpk(lo + bflo(xv[e]) + bflo(bv[e]), hi + bfhi(xv[e]) + bfhi(bv[e]));
        }
        *(GAS u32x4*)(out + eo) = ov;
    };

    if (g < 2) {
        const u32 tO2 = tO ^ 128u;
        acc = zero16;
#define LSTEP(SG, K0) do { RD(SG, tO2); \
        acc = MFMA32(Bc0, Hf[K0], acc); acc = MFMA32(Bc1, Hf[(K0) + 1], acc); ROT(); \
        if constexpr ((SG) < 80) RD((SG) + 1, tO); \
        acc = MFMA32(Bc0, Hf[(K0) + 2], acc); acc = MFMA32(Bc1, Hf[(K0) + 3], acc); ROT(); } while (0)
        LSTEP(76, 0); LSTEP(77, 4); LSTEP(78, 8);
        RD(80, tO); acc = MFMA32(Bc0, Hf[12], acc); acc = MFMA32(Bc1, Hf[13], acc); ROT();
        LSTEP(80, 14);
        char* o6 = wsO + (size_t)(((2 * w) * 7 + 6) * 8 + s) * 1024;
        tile_st(acc, o6, o6 + 7 * 8192);
        STAMPR(10);
        waitvm<2>(); flag_st(w, 2);
        waitvm<1>(); flag_st(2 * w, 3); flag_st(2 * w + 1, 3);
        STAMPR(11);
    } else {
        waitvm<0>(); flag_st(w, 2);
        STAMPR(10);
        finish_unit(w - 4);
        STAMPR(11);
        if (w < 7) finish_unit(w);
        else if (blockIdx.x == 0 && lane == 0) st8c(ws, 0u, (u32x2){tag, (u32)(tag64 >> 32)});
    }
    STAMPR(12);
    { u64 cy1; asm volatile("s_memtime %0\n\ts_waitcnt lgkmcnt(0)" : "=s"(cy1) :: "memory"); tsv[15] = cy1 - cy0; }
    if (lane == 0) for (int i = 0; i < 16; ++i) ((GAS u64*)(ws + W_TS))[((size_t)blockIdx.x * 8 + w) * 16 + i] = tsv[i];
}

__global__ void k_report(const u64* ts) {
    const int l = threadIdx.x;
    u64 t0 = ~0ull;
    for (int w = l; w < 2048; w += 64) { u64 v = ts[w * 16]; t0 = v < t0 ? v : t0; }
    for (int o = 32; o; o >>= 1) { u64 v = __shfl_xor(t0, o); t0 = v < t0 ? v : t0; }
    float av[13], mx[13];
#pragma unroll
    for (int i = 0; i < 13; ++i) {
        float s = 0.f, m = 0.f;
        for (int w = l; w < 2048; w += 64) { float d = (float)(ts[w * 16 + i] - t0) * 0.01f; s += d; m = d > m ? d : m; }
        for (int o = 32; o; o >>= 1) { s += __shfl_xor(s, o); float v = __shfl_xor(m, o); m = v > m ? v : m; }
        av[i] = s / 2048.f; mx[i] = m;
    }
    float ck = (float)ts[15] / ((float)(ts[12] - ts[0]) * 10.f);
    float v = WHICH == 0 ? ck * 5.f : (WHICH == 1 ? mx[0] : (WHICH < 20 ? av[WHICH % 20 - 1] : mx[WHICH % 20 - 1]));
    u64 c0 = __builtin_amdgcn_s_memrealtime(); u64 dt = (u64)(v * 2000.f); dt = dt > 200000 ? 200000 : dt;
    for (int i = 0; i < 1000000; ++i) { if (__builtin_amdgcn_s_memrealtime() - c0 >= dt) break; __builtin_amdgcn_s_sleep(1); }
}
extern "C" void kernel_launch(void* const* d_in, const int* in_sizes, int n_in, void* d_out, int out_size, void* d_ws, size_t ws_size,
                              hipStream_t stream) {
    (void)in_sizes; (void)n_in; (void)out_size; (void)ws_size;
    (void)hipFuncSetAttribute((const void*)fused_block, hipFuncAttributeMaxDynamicSharedMemorySize, LDS_BYTES);
    hipLaunchKernelGGL(fused_block, dim3(256), dim3(512), LDS_BYTES, stream,
                       (const u16*)d_in[0], (const u16*)d_in[1], (const u16*)d_in[2], (const u16*)d_in[4], (const u16*)d_in[5],
                       (const u16*)d_in[6], (const u16*)d_in[7], (u16*)d_out, (char*)d_ws);
    hipLaunchKernelGGL(k_report, dim3(1), dim3(64), 0, stream, (const u64*)((char*)d_ws + W_TS));
}
```

```cpp
#define DIAG
#define HDIAG
#define WHICH 42
#pragma clang diagnostic ignored "-Wint-to-pointer-cast"
#include <hip/hip_runtime.h>
#include <stdint.h>

typedef unsigned short u16;
typedef unsigned u32;
typedef unsigned long long u64;
using bf16x8 = __attribute__((ext_vector_type(8))) short;
using s16x4 = __attribute__((ext_vector_type(4))) short;
using f32x16 = __attribute__((ext_vector_type(16))) float;
using f32x4 = __attribute__((ext_vector_type(4))) float;
using f32x2 = __attribute__((ext_vector_type(2))) float;
using u32x4 = __attribute__((ext_vector_type(4))) unsigned;
using u32x2 = __attribute__((ext_vector_type(2))) unsigned;
#define LAS __attribute__((address_space(3)))
#define GAS __attribute__((address_space(1)))
#define DEV __device__ __forceinline__

#define BSL 4
#define H1I 30
#define H1C 36
#define H2I 48
#define H2C 54
#define FL01 74

namespace {
constexpr int DM = 448, FF = 1792;
constexpr int NSLAB = 81, RING = 12, SLABB = 8192;
constexpr int L_XN = RING * SLABB;
constexpr int XS = 912, HS = 592;
constexpr int L_H = L_XN;
constexpr int L_KVX = L_XN + 64 * HS;
constexpr int L_RED = L_KVX + 16384;
constexpr int L_MISC = L_XN + 64 * XS;
constexpr int L_R2 = L_MISC;
constexpr int L_B1 = L_MISC + 256;
constexpr int L_KVB = L_B1 + 1024;
constexpr int L_DUMMY = L_KVB + 5120;
constexpr int LDS_BYTES = L_DUMMY + 256;
static_assert(L_RED + 2048 <= L_MISC, "lds");
static_assert(LDS_BYTES <= 163840, "lds");
constexpr size_t W_E = 0, W_FL = 4096, W_P = 0x20000, W_R = W_P + (4u << 20), W_O = W_R + 65536;
constexpr size_t W_TS = 32u << 20;
constexpr u32 SPIN_CAP = 1u << 18;

typedef __bf16 bf16x2_t __attribute__((ext_vector_type(2)));
DEV u32 cvtpk(float lo, float hi) { f32x2 v = {lo, hi}; return __builtin_bit_cast(u32, __builtin_convertvector(v, bf16x2_t)); }
DEV float bflo(u32 v) { return __uint_as_float(v << 16); }
DEV float bfhi(u32 v) { return __uint_as_float(v & 0xffff0000u); }
template <int CTRL> DEV float dpp(float v) { return __int_as_float(__builtin_amdgcn_update_dpp(0, __float_as_int(v), CTRL, 0xf, 0xf, true)); }
template <int IMM> DEV void glds16(const char* sb, u32 voff, u32 ldsw) {
    asm volatile("s_add_u32 m0, %2, %3\n\ts_nop 0\n\tglobal_load_lds_dwordx4 %0, %1" :: "v"(voff), "s"(sb), "s"(ldsw), "n"(IMM) : "memory", "scc");
}
template <int IMM> DEV void glds16c(const char* sb, u32 voff, u32 ldsw) {
    asm volatile("s_add_u32 m0, %2, %3\n\ts_nop 0\n\tglobal_load_lds_dwordx4 %0, %1 sc1" :: "v"(voff), "s"(sb), "s"(ldsw), "n"(IMM) : "memory", "scc");
}
DEV void touch(const char* sb, u32 voff, u32 lds) {
    asm volatile("s_mov_b32 m0, %2\n\ts_nop 0\n\tglobal_load_lds_dword %0, %1" :: "v"(voff), "s"(sb), "s"(lds) : "memory");
}
DEV void st16c(char* sb, u32 voff, u32x4 v) { asm volatile("global_store_dwordx4 %0, %1, %2 sc1\n\ts_nop 1" :: "v"(voff), "v"(v), "s"(sb) : "memory"); }
DEV void st8c(char* sb, u32 voff, u32x2 v) { asm volatile("global_store_dwordx2 %0, %1, %2 sc1" :: "v"(voff), "v"(v), "s"(sb) : "memory"); }
template <int N> DEV void waitvm() { asm volatile("s_waitcnt vmcnt(%0)" :: "n"(N) : "memory"); }
template <typename T> DEV T lds_ld(u32 a) { return *(const LAS T*)(a); }
template <typename T> DEV void lds_st(u32 a, T v) { *(LAS T*)(a) = v; }
typedef short v4i16_t __attribute__((ext_vector_type(4)));
DEV bf16x8 trfrag(u32 a) {
    s16x4 lo = __builtin_amdgcn_ds_read_tr16_b64_v4i16((LAS v4i16_t*)(a));
    s16x4 hi = __builtin_amdgcn_ds_read_tr16_b64_v4i16((LAS v4i16_t*)(a + 1024));
    return __builtin_shufflevector(lo, hi, 0, 1, 2, 3, 4, 5, 6, 7);
}
#define MFMA32(a, b, c) __builtin_amdgcn_mfma_f32_32x32x16_bf16(a, b, c, 0, 0, 0)
#define MFMA16(a, b, c) __builtin_amdgcn_mfma_f32_16x16x32_bf16(a, b, c, 0, 0, 0)
#define SBAR() __builtin_amdgcn_sched_barrier(0)

DEV float phi(float t) { float e = __builtin_amdgcn_exp2f(t * 1.4426950408889634f); return (t > 0.f ? t + 1.f : e) + 1e-6f; }
DEV float gelu(float u) {
    const float k0 = -2.f * 0.7978845608028654f * 1.4426950408889634f, k1 = k0 * 0.044715f;
    float p = __builtin_fmaf(u * u, k1, k0);
    float e = __builtin_amdgcn_exp2f(u * p);
    return u * __builtin_amdgcn_rcpf(1.f + e);
}

constexpr int nglds(int s) { return s < 2 ? 0 : ((s + 10 < NSLAB) + (s + 11 < NSLAB)); }
constexpr int extra(int s) { return (s == 16 ? 2 : 0) + (s == H1I ? 2 : 0) + (s == H2I ? 1 : 0) + (s == 64 ? 2 : 0) + (s == 66 ? 2 : 0) + (s == FL01 ? 2 : 0); }
constexpr int younger_than_extra(int from, int upto) { int n = 0; for (int i = from + 2; i < upto; i += 2) n += nglds(i) + extra(i); return n; }
constexpr int vmwait(int s) {
    if (s + 3 <= 11) { int n = 11 - (s + 3); for (int i = 0; i < s; i += 2) n += nglds(i) + extra(i); return n; }
    int last = (s + 3 < NSLAB) ? s - 8 : 70;
    return extra(last) + younger_than_extra(last, s);
}

struct Ctx {
    const char *gKV, *gW1a, *gW1b, *gQ, *gW2, *gWo;
    u32 vKV, vQ, vW1, vW2, vWo1, vL, vLd, vLo;
    u32 ldsw;
};
template <int SG> DEV void issue(const Ctx& c) {
    constexpr int IMM = (SG % RING) * SLABB;
    if constexpr (SG < 14) glds16<IMM>(c.gKV + SG * 32 * 2688, c.vKV, c.ldsw);
    else if constexpr (SG < 28) glds16<IMM>(c.gW1a + (SG - 14) * 32 * 3584, c.vW1, c.ldsw);
    else if constexpr (SG < 42) glds16<IMM>(c.gW1b + (SG - 28) * 32 * 3584, c.vW1, c.ldsw);
    else if constexpr (SG < 49) glds16<IMM>(c.gQ + (SG - 42) * 32 * 2688, c.vQ, c.ldsw);
    else if constexpr (SG < 56) glds16<IMM>(c.gW2 + (SG - 49) * 32 * 896, c.vW2, c.ldsw);
    else if constexpr (SG < 63) glds16<IMM>(c.gW2 + (SG - 56) * 32 * 896 + 256, c.vW2, c.ldsw);
    else if constexpr (SG == 63) glds16<IMM>(c.gWo, c.vW2, c.ldsw);
    else if constexpr (SG == 64) glds16<IMM>(c.gWo, c.vWo1, c.ldsw);
    else if constexpr (SG == 65) glds16<IMM>(c.gWo + 256, c.vW2, c.ldsw);
    else if constexpr (SG == 66) glds16<IMM>(c.gWo + 256, c.vWo1, c.ldsw);
    else if constexpr (SG < 74) glds16<IMM>(c.gW2 + (SG - 67) * 32 * 896 + 512, c.vW2, c.ldsw);
    else if constexpr (SG == 74) glds16<IMM>(c.gWo + 512, c.vW2, c.ldsw);
    else if constexpr (SG == 75) glds16<IMM>(c.gWo + 512, c.vWo1, c.ldsw);
    else if constexpr (SG < 79) glds16<IMM>(c.gW2 + (SG - 76) * 64 * 896 + 768, c.vL, c.ldsw);
    else if constexpr (SG == 79) glds16<IMM>(c.gW2 + 192 * 896 + 768, c.vLd, c.ldsw);
    else glds16<IMM>(c.gWo + 768, c.vLo, c.ldsw);
}
template <int SG, int VM, int LG> DEV void sync_raw(const Ctx& c) {
    SBAR();
    asm volatile("s_waitcnt vmcnt(%0) lgkmcnt(%1)\n\ts_barrier" :: "n"(VM), "n"(LG) : "memory");
    if constexpr (SG >= 2 && SG + 10 < NSLAB) issue<SG + 10>(c);
    if constexpr (SG >= 2 && SG + 11 < NSLAB) issue<SG + 11>(c);
    SBAR();
}
constexpr u32 slot_off(int sg) { return (u32)((sg % RING) * SLABB); }
}

#define STAMP(i) do { if (lane == 0) { SBAR(); ((GAS u64*)(ws + W_TS))[((size_t)blockIdx.x * 8 + w) * 16 + (i)] = __builtin_amdgcn_s_memrealtime(); SBAR(); } } while (0)
#define STAMPR(i) do { SBAR(); asm volatile("s_memrealtime %0\n\ts_waitcnt lgkmcnt(0)" : "=s"(tsv[i]) :: "memory"); SBAR(); } while (0)

__global__ __launch_bounds__(512, 2) void fused_block(const u16* __restrict__ x, const u16* __restrict__ wqkv, const u16* __restrict__ wo,
                                                      const u16* __restrict__ w1, const u16* __restrict__ b1, const u16* __restrict__ w2,
                                                      const u16* __restrict__ b2, u16* __restrict__ out, char* __restrict__ ws) {
    constexpr u32 L0 = 0;
    asm volatile("" :: "s"(x), "s"(wqkv), "s"(wo), "s"(w1), "s"(b1), "s"(w2), "s"(b2), "s"(out), "s"(ws));
    const int lane = threadIdx.x & 63;
    const int w = __builtin_amdgcn_readfirstlane(threadIdx.x >> 6);
    const int th = w & 1, g = w >> 1;
    const int xcd = blockIdx.x & 7, jj = blockIdx.x >> 3;
    const int s = (xcd % (8 / BSL)) * BSL + (jj % BSL);
    const int t = (xcd / (8 / BSL)) * (32 / BSL) + jj / BSL;
    u64 tsv[16];
    for (int i = 0; i < 16; ++i) tsv[i] = 0;
    STAMPR(0);
    u64 cy0; asm volatile("s_memtime %0\n\ts_waitcnt lgkmcnt(0)" : "=s"(cy0) :: "memory");

    u32x4 xr[7];
    const int xrow = 8 * w + (lane >> 3);
    const int rot = (jj % BSL) * (8 / BSL);
    {
        const char* xb = (const char*)x + (size_t)t * (64 * 896);
        const u32 vx = xrow * 896 + (lane & 7) * 16;
#pragma unroll
        for (int j = 0; j < 7; ++j) {
            int jr = j + rot; jr = jr >= 7 ? jr - 7 : jr;
            asm volatile("global_load_dwordx4 %0, %1, %2" : "=v"(xr[j]) : "v"(vx), "s"(xb + jr * 128));
        }
    }
    Ctx c;
    {
        const int r = lane >> 4, ch = (lane & 15) ^ (r << 2), seg = ch >> 3, c8 = ch & 7, row = 4 * w + r;
        const int c7 = c8 > 6 ? 6 : c8;
        c.vKV = row * 2688 + seg * 896 + c7 * 16;
        c.vQ = (row + seg * 224) * 2688 + c7 * 16;
        c.vW1 = row * 3584 + ch * 16;
        c.vW2 = row * 896 + ch * 16;
        c.vWo1 = (row + 32 > 55 ? 55 : row + 32) * 896 + ch * 16;
        c.vL = (row + 32 * seg) * 896 + c8 * 16;
        c.vLd = row * 896 + c8 * 16;
        c.vLo = (row + 32 * seg > 55 ? 55 : row + 32 * seg) * 896 + c8 * 16;
        c.gKV = (const char*)wqkv + 896 + 112 * s;
        c.gQ = (const char*)wqkv + 112 * s;
        const int wb = 448 * s - 64 * (s & 1);
        c.gW1a = (const char*)w1 + wb + ((s & 1) ? 256 : 0);
        c.gW1b = (const char*)w1 + wb + ((s & 1) ? 0 : 256);
        c.gW2 = (const char*)w2 + (size_t)s * (224 * 896);
        c.gWo = (const char*)wo + (size_t)s * (56 * 896);
        c.ldsw = L0 + w * 1024;
    }
    constexpr int NPF = 2;
    {
        const int s0 = s - (jj % BSL);
        const int tid = threadIdx.x;
        {
            constexpr int LPS = (BSL == 1) ? 2 : (BSL == 2 ? 3 : 4);
            const int base = (112 * s0) & ~127;
            int i = tid; const int n = 14 * 3 * LPS;
            u32 off;
            const char* sb = (const char*)wqkv;
            i = i < n ? i : n - 1;
            const int m = i / (14 * LPS), rem = i % (14 * LPS), rr = rem / LPS, ll = rem % LPS;
            const int mm = m == 2 ? 0 : m + 1;
            int col = base + ll * 128; col = col > 768 ? 768 : col;
            off = (14 * jj + rr) * 2688 + mm * 896 + col;
            if (w * 64 < n) touch(sb, off, L0 + L_DUMMY);
            else {
                constexpr int LW = (BSL == 1) ? 4 : (7 * BSL / 2);
                constexpr int W0 = (14 * 3 * LPS + 63) / 64;
                int k = tid - W0 * 64; const int n1 = 14 * LW;
                k = k < n1 ? k : n1 - 1;
                const int bw = (448 * s0) & ~127;
                touch((const char*)w1, (14 * jj + k / LW) * 3584 + bw + (k % LW) * 128, L0 + L_DUMMY);
            }
        }
        {
            constexpr int N2 = 49 * BSL, NO = (392 * BSL + 31) / 32;
            if (w == 7) {
                const int wb = 448 * s - 64 * (s & 1);
                glds16<L_B1>((const char*)b1 + wb, (lane & 31) * 16, L0);
            } else if (w >= 5) {
                int k = jj * NO + (tid - 320 < NO ? tid - 320 : NO - 1); k = k < 392 * BSL ? k : 392 * BSL - 1;
                touch((const char*)wo + (size_t)s0 * (56 * 896), k * 128, L0 + L_DUMMY);
            } else {
                int k = tid < N2 ? tid : N2 - 1;
                touch((const char*)w2 + (size_t)s0 * (224 * 896), (jj * N2 + k) * 128, L0 + L_DUMMY);
            }
        }
    }
    issue<0>(c); issue<1>(c); issue<2>(c); issue<3>(c); issue<4>(c); issue<5>(c);
    issue<6>(c); issue<7>(c); issue<8>(c); issue<9>(c); issue<10>(c); issue<11>(c);
    u64 epoch;
    asm volatile("s_load_dwordx2 %0, %1, 0x0" : "=s"(epoch) : "s"(ws));

    asm volatile("s_waitcnt vmcnt(%8) lgkmcnt(0)" : "+v"(xr[0]), "+v"(xr[1]), "+v"(xr[2]), "+v"(xr[3]), "+v"(xr[4]), "+v"(xr[5]), "+v"(xr[6]), "+s"(epoch) : "n"(12 + NPF));
    STAMPR(1);
    const u64 tag64 = epoch + 0x100000001ull;
    const u32 tag = (u32)tag64;
    {
        float s1 = 0.f, s2 = 0.f, s1b = 0.f, s2b = 0.f;
#pragma unroll
        for (int j = 0; j < 7; ++j)
#pragma unroll
            for (int e = 0; e < 4; ++e) {
                float a = bflo(xr[j][e]), b = bfhi(xr[j][e]);
                s1 += a; s2 = __builtin_fmaf(a, a, s2); s1b += b; s2b = __builtin_fmaf(b, b, s2b);
            }
        s1 += s1b; s2 += s2b;
        s1 += dpp<0xB1>(s1); s2 += dpp<0xB1>(s2);
        s1 += dpp<0x4E>(s1); s2 += dpp<0x4E>(s2);
        s1 += dpp<0x141>(s1); s2 += dpp<0x141>(s2);
        const float mean = s1 * (1.f / DM);
        const float var = __builtin_fmaxf(__builtin_fmaf(-mean, mean, s2 * (1.f / DM)), 0.f);
        const float rstd = __builtin_amdgcn_rsqf(var + 1e-5f);
        const float nb = -mean * rstd;
        const u32 xa = L0 + L_XN + xrow * XS + (lane & 7) * 16;
#pragma unroll
        for (int j = 0; j < 7; ++j) {
            int jr = j + rot; jr = jr >= 7 ? jr - 7 : jr;
            u32x4 o;
#pragma unroll
            for (int e = 0; e < 4; ++e) o[e] = cvtpk(__builtin_fmaf(bflo(xr[j][e]), rstd, nb), __builtin_fmaf(bfhi(xr[j][e]), rstd, nb));
            lds_st<u32x4>(xa + jr * 128, o);
        }
        if ((lane & 7) == 0) lds_st<float>(L0 + L_R2 + xrow * 4, __builtin_amdgcn_rsqf(var * rstd * rstd + 1e-5f));
    }
    asm volatile("s_waitcnt lgkmcnt(0)\n\ts_barrier" ::: "memory");
    const int l31 = lane & 31, hf = lane >> 5;
    bf16x8 A[28];
    {
        const u32 a = L0 + L_XN + (32 * th + l31) * XS + hf * 16;
#pragma unroll
        for (int j = 0; j < 28; ++j) A[j] = lds_ld<bf16x8>(a + j * 32);
    }
    const float r2 = lds_ld<float>(L0 + L_R2 + (32 * th + l31) * 4);

    const int blk = (lane >> 4) & 1, q = (lane & 15) >> 2, p = lane & 3;
    const u32 trrow = L0 + (8 * hf + q) * 256;
    const u32 mstd = 32 * blk + 8 * p, mperm = 32 * (p & 1) + 16 * blk + 8 * (p >> 1);
    const u32 tKV = trrow + ((((g >> 1) * 128 + (g & 1) * 56) + mstd) ^ (q << 6));
    const int gq = (s & 1) ? 3 - g : g;
    const u32 tW1 = trrow + ((64 * gq + mperm) ^ (q << 6));
    const u32 tQ = trrow + (((g & 1) * 56 + mperm) ^ (q << 6));
    const u32 tO = trrow + ((64 * g + mperm) ^ (q << 6));
    const u32 hrow = L0 + L_H + (32 * th + l31) * HS;

    char* const wsP = ws + W_P + (size_t)s * (32 * 32 * 512);
    char* const wsR = ws + W_R + (size_t)s * 8192;
    char* const wsO = ws + W_O + (size_t)t * (8 * 7 * 8 * 1024);
    char* const wsF = ws + W_FL + (size_t)t * (8 * 256);

    bf16x8 Bc0, Bc1, Bn0, Bn1;
    f32x16 acc, accP;
    const f32x16 zero16 = {0.f, 0.f, 0.f, 0.f, 0.f, 0.f, 0.f, 0.f, 0.f, 0.f, 0.f, 0.f, 0.f, 0.f, 0.f, 0.f};

    auto hop1_issue = [&]() { const char* src = wsP + (size_t)t * 16384 + w * 2048; glds16c<L_KVX>(src, lane * 16, L0 + w * 2048); glds16c<L_KVX + 1024>(src + 1024, lane * 16, L0 + w * 2048); };
    auto hop1_check = [&]() {
        u32x4 a, b;
        for (u32 spin = 0;; ++spin) {
            a = lds_ld<u32x4>(L0 + L_KVX + w * 2048 + lane * 16); b = lds_ld<u32x4>(L0 + L_KVX + w * 2048 + 1024 + lane * 16);
            const bool ok = a[1] == tag && a[3] == tag && b[1] == tag && b[3] == tag;
            if (__all(ok) || spin >= SPIN_CAP) break;
            hop1_issue(); waitvm<0>();
        }
        float v0 = __uint_as_float(a[0]) + __uint_as_float(b[0]), v1 = __uint_as_float(a[2]) + __uint_as_float(b[2]);
        auto r0 = __builtin_amdgcn_permlane32_swap(__float_as_uint(v0), __float_as_uint(v0), false, false);
        auto r1 = __builtin_amdgcn_permlane32_swap(__float_as_uint(v1), __float_as_uint(v1), false, false);
        f32x2 o = {__uint_as_float(r0[0]) + __uint_as_float(r0[1]), __uint_as_float(r1[0]) + __uint_as_float(r1[1])};
        if (lane < 32) lds_st<f32x2>(L0 + L_RED + w * 256 + lane * 8, o);
    };
    auto hop1_publish = [&]() {
        if (w == 6) {
            f32x2 v = lds_ld<f32x2>(L0 + L_RED + l31 * 8);
#pragma unroll
            for (int i = 1; i < 8; ++i) v += lds_ld<f32x2>(L0 + L_RED + i * 256 + l31 * 8);
            if (lane < 32) st8c(wsR + t * 256, lane * 8, (u32x2){cvtpk(v[0], v[1]), tag});
        }
    };
    auto hop2_issue = [&]() { glds16c<L_KVX>(wsR + w * 1024, lane * 16, L0 + w * 2048); };
    auto hop2_check = [&]() {
        u32x4 a;
        for (u32 spin = 0;; ++spin) {
            a = lds_ld<u32x4>(L0 + L_KVX + w * 2048 + lane * 16);
            const bool ok = a[1] == tag && a[3] == tag;
            if (__all(ok) || spin >= SPIN_CAP) break;
            hop2_issue(); waitvm<0>();
        }
        const int Ln = 32 * ((lane >> 4) & 1) + 2 * (lane & 15);
        const u32 ad = L0 + L_KVB + (w >> 2) * 2560 + (16 * (w & 1) + (Ln & 15)) * 80 + (16 * ((w >> 1) & 1) + 4 * (Ln >> 4) + 2 * hf) * 2;
        lds_st<u32>(ad, a[0]); lds_st<u32>(ad + 80, a[2]);
    };
#define SYNC(SG, LG) do { \
        constexpr bool full_ = ((SG) == 16 || (SG) == H1C + 2 || (SG) == 48 || (SG) == H2C + 2 || (SG) == H2C + 4); \
        constexpr int vmr_ = vmwait(SG); \
        constexpr int vm1_ = (SG) == H1C ? younger_than_extra(H1I, SG) : ((SG) == H2C ? younger_than_extra(H2I, SG) : ((SG) == FL01 ? younger_than_extra(66, SG) : 63)); \
        sync_raw<SG, (vm1_ < vmr_ ? vm1_ : vmr_), (full_ ? 0 : (LG))>(c); \
        if constexpr ((SG) == H1I) hop1_issue(); \
        if constexpr ((SG) == H2I) hop2_issue(); \
        if constexpr ((SG) == H1C) { hop1_check(); STAMPR(5); } \
        if constexpr ((SG) == H1C + 2) hop1_publish(); \
        if constexpr ((SG) == H2C) { hop2_check(); STAMPR(7); } \
    } while (0)
#define RD(SG, T) do { Bn0 = trfrag((T) + slot_off(SG)); Bn1 = trfrag((T) + slot_off(SG) + 4096); } while (0)
#define ROT() do { Bc0 = Bn0; Bc1 = Bn1; } while (0)

    u32 hp[8];
    u32x4 bq0, bq1;
    auto gelu2 = [&](int i) {
        const u32 bb = i < 4 ? bq0[i & 3] : bq1[i & 3];
        hp[i] = cvtpk(gelu(__builtin_fmaf(accP[2 * i], r2, bflo(bb))), gelu(__builtin_fmaf(accP[2 * i + 1], r2, bfhi(bb))));
    };
    auto bias_ld = [&](int wblk) { const u32 a = L0 + L_B1 + wblk * 64 + hf * 32; bq0 = lds_ld<u32x4>(a); bq1 = lds_ld<u32x4>(a + 16); };
    auto h_st = [&](int wblk, int half, bool live) {
        const u32 a = live ? hrow + (wblk - (s & 1)) * 64 + hf * 32 + half * 16 : L0 + L_DUMMY;
        lds_st<u32x4>(a, (u32x4){hp[4 * half], hp[4 * half + 1], hp[4 * half + 2], hp[4 * half + 3]});
    };

    SYNC(0, 0);
    STAMPR(2);
    RD(0, tKV); ROT();
    acc = zero16;
#define G1STEP(SG) do { if constexpr ((SG) % 2 == 0 && (SG) > 0) SYNC(SG, 4); \
        if constexpr ((SG) < 13) RD((SG) + 1, tKV); else RD((SG) + 1, tW1); \
        acc = MFMA32(A[2 * (SG)], Bc0, acc); acc = MFMA32(A[2 * (SG) + 1], Bc1, acc); ROT(); } while (0)
    G1STEP(0); G1STEP(1); G1STEP(2); G1STEP(3); G1STEP(4); G1STEP(5); G1STEP(6);
    G1STEP(7); G1STEP(8); G1STEP(9); G1STEP(10); G1STEP(11); G1STEP(12); G1STEP(13);
    accP = acc; acc = zero16;
    STAMPR(3);

#define W1STEP(SG, B0, TN) do { RD((SG) + 1, TN); \
        acc = MFMA32(Bc0, A[2 * ((SG) - (B0))], acc); acc = MFMA32(Bc1, A[2 * ((SG) - (B0)) + 1], acc); ROT(); } while (0)
    SYNC(14, 4);
    W1STEP(14, 14, tW1);
    {
        u32 pk[8];
        if (g < 2) {
#pragma unroll
            for (int i = 0; i < 8; ++i) { u32 v = cvtpk(phi(accP[2 * i]), phi(accP[2 * i + 1])); pk[i] = l31 < 28 ? v : 0u; }
        } else {
#pragma unroll
            for (int i = 0; i < 8; ++i) { u32 v = cvtpk(accP[2 * i], accP[2 * i + 1]); pk[i] = l31 == 28 ? 0x3f803f80u : v; }
        }
        const u32 a = L0 + L_KVX + (g * 8 + th * 4 + hf * 2) * 512 + l31 * 16;
        lds_st<u32x4>(a, (u32x4){pk[0], pk[1], pk[2], pk[3]});
        lds_st<u32x4>(a + 512, (u32x4){pk[4], pk[5], pk[6], pk[7]});
        if (w == 7) lds_st<u32x4>(L0 + L_H + lane * HS + 560, (u32x4){0u, 0u, 0u, 0u});
    }
    W1STEP(15, 14, tW1);
    SYNC(16, 4);
    {
        const u32 ka = L0 + L_KVX + ((w >> 2) * 8 + (lane >> 4)) * 512 + (16 * ((w >> 1) & 1) + (lane & 15)) * 16;
        const u32 va = L0 + L_KVX + ((2 + (w >> 2)) * 8 + (lane >> 4)) * 512 + (16 * (w & 1) + (lane & 15)) * 16;
        f32x4 kv = {0.f, 0.f, 0.f, 0.f};
        kv = MFMA16(lds_ld<bf16x8>(ka), lds_ld<bf16x8>(va), kv);
        kv = MFMA16(lds_ld<bf16x8>(ka + 2048), lds_ld<bf16x8>(va + 2048), kv);
        const u32 vo = ((4 * w + hf) * 32 + t) * 512 + l31 * 16;
        st16c(wsP, vo, (u32x4){__float_as_uint(kv[0]), tag, __float_as_uint(kv[1]), tag});
        st16c(wsP + 2 * 16384, vo, (u32x4){__float_as_uint(kv[2]), tag, __float_as_uint(kv[3]), tag});
    }
    STAMPR(4);
    W1STEP(16, 14, tW1); W1STEP(17, 14, tW1);
#define W1PAIR(SG, B0) do { SYNC(SG, 4); W1STEP(SG, B0, tW1); W1STEP((SG) + 1, B0, tW1); } while (0)
    W1PAIR(18, 14); W1PAIR(20, 14); W1PAIR(22, 14); W1PAIR(24, 14); W1PAIR(26, 14);
    accP = acc; acc = zero16;

    const int wbA = (s & 1) ? 7 - g : g, wbB = (s & 1) ? 3 - g : 4 + g;
    SYNC(28, 4); bias_ld(wbA); W1STEP(28, 28, tW1); W1STEP(29, 28, tW1); gelu2(0);
    SYNC(30, 4); W1STEP(30, 28, tW1); gelu2(1); W1STEP(31, 28, tW1);
    SYNC(32, 4); W1STEP(32, 28, tW1); gelu2(2); W1STEP(33, 28, tW1); gelu2(3);
    SYNC(34, 4); h_st(wbA, 0, true); W1STEP(34, 28, tW1); gelu2(4); W1STEP(35, 28, tW1);
    SYNC(36, 4); W1STEP(36, 28, tW1); gelu2(5); W1STEP(37, 28, tW1);
    SYNC(38, 4); W1STEP(38, 28, tW1); gelu2(6); W1STEP(39, 28, tW1);
    SYNC(40, 4); W1STEP(40, 28, tW1); gelu2(7); h_st(wbA, 1, true); W1STEP(41, 28, tQ);
    accP = acc; acc = zero16;
    STAMPR(6);

    bf16x8 Hf[18];
    u32 pq[8];
    if (g < 2) {
        SYNC(42, 4); bias_ld(wbB); gelu2(0); gelu2(1); gelu2(2);
        SYNC(44, 4); gelu2(3); h_st(wbB, 0, true); gelu2(4); gelu2(5);
        SYNC(46, 4); gelu2(6); gelu2(7); h_st(wbB, 1, true);
        SYNC(48, 4);
        RD(49, tO); ROT();
#pragma unroll
        for (int j = 0; j < 14; ++j) Hf[j] = lds_ld<bf16x8>(hrow + hf * 16 + j * 32);
#pragma unroll
        for (int i = 0; i < 8; ++i) pq[i] = 0;
    } else {
        const u32 tQ2 = tQ ^ 128u;
#define QSTEP(SG, TN) do { RD(SG, tQ2); \
        acc = MFMA32(Bc0, A[2 * ((SG) - 42)], acc); acc = MFMA32(Bc1, A[2 * ((SG) - 42) + 1], acc); ROT(); \
        RD((SG) + 1, TN); \
        acc = MFMA32(Bc0, A[14 + 2 * ((SG) - 42)], acc); acc = MFMA32(Bc1, A[15 + 2 * ((SG) - 42)], acc); ROT(); } while (0)
        SYNC(42, 4); bias_ld(wbB); QSTEP(42, tQ); gelu2(0); QSTEP(43, tQ); gelu2(1); gelu2(2);
        SYNC(44, 4); QSTEP(44, tQ); gelu2(3); h_st(wbB, 0, g == 2); gelu2(4); QSTEP(45, tQ); gelu2(5);
        SYNC(46, 4); QSTEP(46, tQ); gelu2(6); gelu2(7); h_st(wbB, 1, g == 2); QSTEP(47, tQ);
        SYNC(48, 4);
        QSTEP(48, tO);
#pragma unroll
        for (int j = 0; j < 14; ++j) Hf[j] = lds_ld<bf16x8>(hrow + hf * 16 + j * 32);
#pragma unroll
        for (int i = 0; i < 8; ++i) pq[i] = cvtpk(phi(acc[2 * i]), phi(acc[2 * i + 1]));
    }
    acc = zero16; accP = zero16;

    auto attn_o = [&]() {
        if (g >= 2) {
            const int m = l31, e = 16 * ((m >> 2) & 1) + 4 * (m >> 3) + (m & 3);
            const u32 ka = L0 + L_KVB + (g & 1) * 2560 + e * 80 + hf * 32;
            f32x16 nm = MFMA32(lds_ld<bf16x8>(ka), __builtin_bit_cast(bf16x8, (u32x4){pq[0], pq[1], pq[2], pq[3]}), zero16);
            nm = MFMA32(lds_ld<bf16x8>(ka + 16), __builtin_bit_cast(bf16x8, (u32x4){pq[4], pq[5], pq[6], pq[7]}), nm);
            auto dd = __builtin_amdgcn_permlane32_swap(__float_as_uint(nm[12]), __float_as_uint(nm[12]), false, false);
            const float rd = __builtin_amdgcn_rcpf(__uint_as_float(dd[1]) + 1e-6f);
            const u32 oa = hrow + 448 + (g & 1) * 56 + hf * 32;
#pragma unroll
            for (int i = 0; i < 4; ++i) {
                u32x2 v = {cvtpk(nm[4 * i] * rd, nm[4 * i + 1] * rd), cvtpk(nm[4 * i + 2] * rd, nm[4 * i + 3] * rd)};
                if (i < 3 || hf == 0) lds_st<u32x2>(oa + i * 8, v);
            }
        }
    };
    auto tile_st = [&](const f32x16& a, char* p0, char* p1) {
        st16c(p0, lane * 16, (u32x4){cvtpk(a[0], a[1]), cvtpk(a[2], a[3]), cvtpk(a[4], a[5]), cvtpk(a[6], a[7])});
        st16c(p1, lane * 16, (u32x4){cvtpk(a[8], a[9]), cvtpk(a[10], a[11]), cvtpk(a[12], a[13]), cvtpk(a[14], a[15])});
    };
    auto flag_st = [&](int dest, int j) { if (lane == 0) st8c(wsF + dest * 256 + j * 64 + s * 8, 0u, (u32x2){tag, (u32)(tag64 >> 32)}); };
    char* const otile = wsO + (size_t)(w * 7 * 8 + s) * 1024;
#define OSTEP(SG, K0, ACC) do { \
        if constexpr ((SG) % 2 == 0) { SYNC(SG, 4); \
            if constexpr ((SG) == H2C + 2) attn_o(); \
            if constexpr ((SG) == H2C + 4) { _Pragma("unroll") for (int j = 14; j < 18; ++j) Hf[j] = lds_ld<bf16x8>(hrow + hf * 16 + j * 32); } \
            if constexpr ((SG) == FL01) { flag_st(w, 0); flag_st(w, 1); } } \
        RD((SG) + 1, tO); \
        ACC = MFMA32(Bc0, Hf[K0], ACC); ACC = MFMA32(Bc1, Hf[(K0) + 1], ACC); ROT(); } while (0)
    static_assert(H2C + 4 <= 62 && H2C >= 50 && H2I >= H1C + 4 && H1I >= 18, "schedule");
    OSTEP(49, 0, acc); OSTEP(50, 2, acc); OSTEP(51, 4, acc); OSTEP(52, 6, acc); OSTEP(53, 8, acc); OSTEP(54, 10, acc); OSTEP(55, 12, acc);
    OSTEP(56, 0, accP); OSTEP(57, 2, accP); OSTEP(58, 4, accP); OSTEP(59, 6, accP); OSTEP(60, 8, accP); OSTEP(61, 10, accP); OSTEP(62, 12, accP);
    STAMPR(8);
    OSTEP(63, 14, acc); OSTEP(64, 16, acc);
    OSTEP(65, 14, accP);
    tile_st(acc, otile, otile + 8192); acc = zero16;
    OSTEP(66, 16, accP);
    OSTEP(67, 0, acc);
    tile_st(accP, otile + 2 * 8192, otile + 3 * 8192);
    OSTEP(68, 2, acc); OSTEP(69, 4, acc); OSTEP(70, 6, acc); OSTEP(71, 8, acc); OSTEP(72, 10, acc); OSTEP(73, 12, acc);
    OSTEP(74, 14, acc); OSTEP(75, 16, acc);
    sync_raw<76, (FL01 == 74 ? 2 : 0), 0>(c);
    STAMPR(9);
    tile_st(acc, otile + 4 * 8192, otile + 5 * 8192);

    auto finish_unit = [&](int u) {
        const int j = u >> 1;
        int tk, col;
        if (u < 6) { tk = 32 * (s & 1) + l31; col = 128 * j + 32 * (s >> 1) + 16 * hf + 8 * (u & 1); }
        else { tk = 32 * ((s >> 1) & 1) + l31; col = 384 + 32 * (s >> 2) + 16 * hf + 8 * (s & 1); }
        const size_t eo = (size_t)(64 * t + tk) * DM + col;
        const u32x4 xv = *(const GAS u32x4*)(x + eo);
        const u32x4 bv = *(const GAS u32x4*)(b2 + col);
        const GAS u64* fl = (const GAS u64*)(wsF + s * 256 + j * 64) + (lane & 7);
        for (u32 spin = 0; spin < SPIN_CAP; ++spin) {
            const u64 f = __hip_atomic_load(fl, __ATOMIC_RELAXED, __HIP_MEMORY_SCOPE_AGENT);
            if (__all(f == tag64)) break;
        }
        const char* src = wsO + (size_t)((s * 7 + u) * 8) * 1024;
        u32x4 pv[8];
        const u32 vo = lane * 16;
        asm volatile("global_load_dwordx4 %0, %8, %9 sc1\n\tglobal_load_dwordx4 %1, %8, %9 offset:1024 sc1\n\t"
                     "global_load_dwordx4 %2, %8, %9 offset:2048 sc1\n\tglobal_load_dwordx4 %3, %8, %9 offset:3072 sc1\n\t"
                     "global_load_dwordx4 %4, %8, %10 sc1\n\tglobal_load_dwordx4 %5, %8, %10 offset:1024 sc1\n\t"
                     "global_load_dwordx4 %6, %8, %10 offset:2048 sc1\n\tglobal_load_dwordx4 %7, %8, %10 offset:3072 sc1\n\t"
                     "s_waitcnt vmcnt(0)"
                     : "=&v"(pv[0]), "=&v"(pv[1]), "=&v"(pv[2]), "=&v"(pv[3]), "=&v"(pv[4]), "=&v"(pv[5]), "=&v"(pv[6]), "=&v"(pv[7])
                     : "v"(vo), "s"(src), "s"(src + 4096) : "memory");
        u32x4 ov;
#pragma unroll
        for (int e = 0; e < 4; ++e) {
            float lo = bflo(pv[0][e]), hi = bfhi(pv[0][e]);
#pragma unroll
            for (int i = 1; i < 8; ++i) { lo += bflo(pv[i][e]); hi += bfhi(pv[i][e]); }
            ov[e] = cvtpk(lo + bflo(xv[e]) + bflo(bv[e]), hi + bfhi(xv[e]) + bfhi(bv[e]));
        }
        *(GAS u32x4*)(out + eo) = ov;
    };

    if (g < 2) {
        const u32 tO2 = tO ^ 128u;
        acc = zero16;
#define LSTEP(SG, K0) do { RD(SG, tO2); \
        acc = MFMA32(Bc0, Hf[K0], acc); acc = MFMA32(Bc1, Hf[(K0) + 1], acc); ROT(); \
        if constexpr ((SG) < 80) RD((SG) + 1, tO); \
        acc = MFMA32(Bc0, Hf[(K0) + 2], acc); acc = MFMA32(Bc1, Hf[(K0) + 3], acc); ROT(); } while (0)
        LSTEP(76, 0); LSTEP(77, 4); LSTEP(78, 8);
        RD(80, tO); acc = MFMA32(Bc0, Hf[12], acc); acc = MFMA32(Bc1, Hf[13], acc); ROT();
        LSTEP(80, 14);
        char* o6 = wsO + (size_t)(((2 * w) * 7 + 6) * 8 + s) * 1024;
        tile_st(acc, o6, o6 + 7 * 8192);
        STAMPR(10);
        waitvm<2>(); flag_st(w, 2);
        waitvm<1>(); flag_st(2 * w, 3); flag_st(2 * w + 1, 3);
        STAMPR(11);
    } else {
        waitvm<0>(); flag_st(w, 2);
        STAMPR(10);
        finish_unit(w - 4);
        STAMPR(11);
        if (w < 7) finish_unit(w);
        else if (blockIdx.x == 0 && lane == 0) st8c(ws, 0u, (u32x2){tag, (u32)(tag64 >> 32)});
    }
    STAMPR(12);
    { u64 cy1; asm volatile("s_memtime %0\n\ts_waitcnt lgkmcnt(0)" : "=s"(cy1) :: "memory"); tsv[15] = cy1 - cy0; }
    if (lane == 0) for (int i = 0; i < 16; ++i) ((GAS u64*)(ws + W_TS))[((size_t)blockIdx.x * 8 + w) * 16 + i] = tsv[i];
}

__global__ void k_pre(u64* ts) { ts[2048 * 16] = __builtin_amdgcn_s_memrealtime(); }
__global__ void k_report(const u64* ts) {
    const u64 tme = __builtin_amdgcn_s_memrealtime();
    const int l = threadIdx.x;
    u64 t0 = ~0ull;
    for (int w = l; w < 2048; w += 64) { u64 v = ts[w * 16]; t0 = v < t0 ? v : t0; }
    for (int o = 32; o; o >>= 1) { u64 v = __shfl_xor(t0, o); t0 = v < t0 ? v : t0; }
    float av[13], mx[13];
#pragma unroll
    for (int i = 0; i < 13; ++i) {
        float s = 0.f, m = 0.f;
        for (int w = l; w < 2048; w += 64) { float d = (float)(ts[w * 16 + i] - t0) * 0.01f; s += d; m = d > m ? d : m; }
        for (int o = 32; o; o >>= 1) { s += __shfl_xor(s, o); float v = __shfl_xor(m, o); m = v > m ? v : m; }
        av[i] = s / 2048.f; mx[i] = m;
    }
    float ck = (float)ts[15] / ((float)(ts[12] - ts[0]) * 10.f);
    float v = WHICH == 0 ? ck * 5.f : (WHICH == 1 ? mx[0] : (WHICH < 20 ? av[(WHICH + 19) % 20 % 13] : mx[(WHICH + 19) % 20 % 13]));
    if (WHICH == 40) v = (float)(tme - t0) * 0.01f - mx[12];
    if (WHICH == 41) v = (float)(t0 - ts[2048 * 16]) * 0.01f;
    if (WHICH == 42) v = mx[12] * 0.001f;
    u64 c0 = __builtin_amdgcn_s_memrealtime(); u64 dt = (u64)(v * 2000.f); dt = dt > 200000 ? 200000 : dt;
    for (int i = 0; i < 1000000; ++i) { if (__builtin_amdgcn_s_memrealtime() - c0 >= dt) break; __builtin_amdgcn_s_sleep(1); }
}
extern "C" void kernel_launch(void* const* d_in, const int* in_sizes, int n_in, void* d_out, int out_size, void* d_ws, size_t ws_size,
                              hipStream_t stream) {
    (void)in_sizes; (void)n_in; (void)out_size; (void)ws_size;
    (void)hipFuncSetAttribute((const void*)fused_block, hipFuncAttributeMaxDynamicSharedMemorySize, LDS_BYTES);
    hipLaunchKernelGGL(fused_block, dim3(256), dim3(512), LDS_BYTES, stream,
                       (const u16*)d_in[0], (const u16*)d_in[1], (const u16*)d_in[2], (const u16*)d_in[4], (const u16*)d_in[5],
                       (const u16*)d_in[6], (const u16*)d_in[7], (u16*)d_out, (char*)d_ws);
    hipLaunchKernelGGL(k_report, dim3(1), dim3(64), 0, stream, (const u64*)((char*)d_ws + W_TS));
}
```

```cpp
#define DIAG
#define HDIAG
#define WHICH 45
#pragma clang diagnostic ignored "-Wint-to-pointer-cast"
#include <hip/hip_runtime.h>
#include <stdint.h>

typedef unsigned short u16;
typedef unsigned u32;
typedef unsigned long long u64;
using bf16x8 = __attribute__((ext_vector_type(8))) short;
using s16x4 = __attribute__((ext_vector_type(4))) short;
using f32x16 = __attribute__((ext_vector_type(16))) float;
using f32x4 = __attribute__((ext_vector_type(4))) float;
using f32x2 = __attribute__((ext_vector_type(2))) float;
using u32x4 = __attribute__((ext_vector_type(4))) unsigned;
using u32x2 = __attribute__((ext_vector_type(2))) unsigned;
#define LAS __attribute__((address_space(3)))
#define GAS __attribute__((address_space(1)))
#define DEV __device__ __forceinline__

#define BSL 4
#define H1I 30
#define H1C 36
#define H2I 48
#define H2C 54
#define FL01 74

namespace {
constexpr int DM = 448, FF = 1792;
constexpr int NSLAB = 81, RING = 12, SLABB = 8192;
constexpr int L_XN = RING * SLABB;
constexpr int XS = 912, HS = 592;
constexpr int L_H = L_XN;
constexpr int L_KVX = L_XN + 64 * HS;
constexpr int L_RED = L_KVX + 16384;
constexpr int L_MISC = L_XN + 64 * XS;
constexpr int L_R2 = L_MISC;
constexpr int L_B1 = L_MISC + 256;
constexpr int L_KVB = L_B1 + 1024;
constexpr int L_DUMMY = L_KVB + 5120;
constexpr int LDS_BYTES = L_DUMMY + 256;
static_assert(L_RED + 2048 <= L_MISC, "lds");
static_assert(LDS_BYTES <= 163840, "lds");
constexpr size_t W_E = 0, W_FL = 4096, W_P = 0x20000, W_R = W_P + (4u << 20), W_O = W_R + 65536;
constexpr size_t W_TS = 32u << 20;
constexpr u32 SPIN_CAP = 1u << 18;

typedef __bf16 bf16x2_t __attribute__((ext_vector_type(2)));
DEV u32 cvtpk(float lo, float hi) { f32x2 v = {lo, hi}; return __builtin_bit_cast(u32, __builtin_convertvector(v, bf16x2_t)); }
DEV float bflo(u32 v) { return __uint_as_float(v << 16); }
DEV float bfhi(u32 v) { return __uint_as_float(v & 0xffff0000u); }
template <int CTRL> DEV float dpp(float v) { return __int_as_float(__builtin_amdgcn_update_dpp(0, __float_as_int(v), CTRL, 0xf, 0xf, true)); }
template <int IMM> DEV void glds16(const char* sb, u32 voff, u32 ldsw) {
    asm volatile("s_add_u32 m0, %2, %3\n\ts_nop 0\n\tglobal_load_lds_dwordx4 %0, %1" :: "v"(voff), "s"(sb), "s"(ldsw), "n"(IMM) : "memory", "scc");
}
template <int IMM> DEV void glds16c(const char* sb, u32 voff, u32 ldsw) {
    asm volatile("s_add_u32 m0, %2, %3\n\ts_nop 0\n\tglobal_load_lds_dwordx4 %0, %1 sc1" :: "v"(voff), "s"(sb), "s"(ldsw), "n"(IMM) : "memory", "scc");
}
DEV void touch(const char* sb, u32 voff, u32 lds) {
    asm volatile("s_mov_b32 m0, %2\n\ts_nop 0\n\tglobal_load_lds_dword %0, %1" :: "v"(voff), "s"(sb), "s"(lds) : "memory");
}
DEV void st16c(char* sb, u32 voff, u32x4 v) { asm volatile("global_store_dwordx4 %0, %1, %2 sc1\n\ts_nop 1" :: "v"(voff), "v"(v), "s"(sb) : "memory"); }
DEV void st8c(char* sb, u32 voff, u32x2 v) { asm volatile("global_store_dwordx2 %0, %1, %2 sc1" :: "v"(voff), "v"(v), "s"(sb) : "memory"); }
template <int N> DEV void waitvm() { asm volatile("s_waitcnt vmcnt(%0)" :: "n"(N) : "memory"); }
template <typename T> DEV T lds_ld(u32 a) { return *(const LAS T*)(a); }
template <typename T> DEV void lds_st(u32 a, T v) { *(LAS T*)(a) = v; }
typedef short v4i16_t __attribute__((ext_vector_type(4)));
DEV bf16x8 trfrag(u32 a) {
    s16x4 lo = __builtin_amdgcn_ds_read_tr16_b64_v4i16((LAS v4i16_t*)(a));
    s16x4 hi = __builtin_amdgcn_ds_read_tr16_b64_v4i16((LAS v4i16_t*)(a + 1024));
    return __builtin_shufflevector(lo, hi, 0, 1, 2, 3, 4, 5, 6, 7);
}
#define MFMA32(a, b, c) __builtin_amdgcn_mfma_f32_32x32x16_bf16(a, b, c, 0, 0, 0)
#define MFMA16(a, b, c) __builtin_amdgcn_mfma_f32_16x16x32_bf16(a, b, c, 0, 0, 0)
#define SBAR() __builtin_amdgcn_sched_barrier(0)

DEV float phi(float t) { float e = __builtin_amdgcn_exp2f(t * 1.4426950408889634f); return (t > 0.f ? t + 1.f : e) + 1e-6f; }
DEV float gelu(float u) {
    const float k0 = -2.f * 0.7978845608028654f * 1.4426950408889634f, k1 = k0 * 0.044715f;
    float p = __builtin_fmaf(u * u, k1, k0);
    float e = __builtin_amdgcn_exp2f(u * p);
    return u * __builtin_amdgcn_rcpf(1.f + e);
}

constexpr int nglds(int s) { return s < 2 ? 0 : ((s + 10 < NSLAB) + (s + 11 < NSLAB)); }
constexpr int extra(int s) { return (s == 16 ? 2 : 0) + (s == H1I ? 2 : 0) + (s == H2I ? 1 : 0) + (s == 64 ? 2 : 0) + (s == 66 ? 2 : 0) + (s == FL01 ? 2 : 0); }
constexpr int younger_than_extra(int from, int upto) { int n = 0; for (int i = from + 2; i < upto; i += 2) n += nglds(i) + extra(i); return n; }
constexpr int vmwait(int s) {
    if (s + 3 <= 11) { int n = 11 - (s + 3); for (int i = 0; i < s; i += 2) n += nglds(i) + extra(i); return n; }
    int last = (s + 3 < NSLAB) ? s - 8 : 70;
    return extra(last) + younger_than_extra(last, s);
}

struct Ctx {
    const char *gKV, *gW1a, *gW1b, *gQ, *gW2, *gWo;
    u32 vKV, vQ, vW1, vW2, vWo1, vL, vLd, vLo;
    u32 ldsw;
};
template <int SG> DEV void issue(const Ctx& c) {
    constexpr int IMM = (SG % RING) * SLABB;
    if constexpr (SG < 14) glds16<IMM>(c.gKV + SG * 32 * 2688, c.vKV, c.ldsw);
    else if constexpr (SG < 28) glds16<IMM>(c.gW1a + (SG - 14) * 32 * 3584, c.vW1, c.ldsw);
    else if constexpr (SG < 42) glds16<IMM>(c.gW1b + (SG - 28) * 32 * 3584, c.vW1, c.ldsw);
    else if constexpr (SG < 49) glds16<IMM>(c.gQ + (SG - 42) * 32 * 2688, c.vQ, c.ldsw);
    else if constexpr (SG < 56) glds16<IMM>(c.gW2 + (SG - 49) * 32 * 896, c.vW2, c.ldsw);
    else if constexpr (SG < 63) glds16<IMM>(c.gW2 + (SG - 56) * 32 * 896 + 256, c.vW2, c.ldsw);
    else if constexpr (SG == 63) glds16<IMM>(c.gWo, c.vW2, c.ldsw);
    else if constexpr (SG == 64) glds16<IMM>(c.gWo, c.vWo1, c.ldsw);
    else if constexpr (SG == 65) glds16<IMM>(c.gWo + 256, c.vW2, c.ldsw);
    else if constexpr (SG == 66) glds16<IMM>(c.gWo + 256, c.vWo1, c.ldsw);
    else if constexpr (SG < 74) glds16<IMM>(c.gW2 + (SG - 67) * 32 * 896 + 512, c.vW2, c.ldsw);
    else if constexpr (SG == 74) glds16<IMM>(c.gWo + 512, c.vW2, c.ldsw);
    else if constexpr (SG == 75) glds16<IMM>(c.gWo + 512, c.vWo1, c.ldsw);
    else if constexpr (SG < 79) glds16<IMM>(c.gW2 + (SG - 76) * 64 * 896 + 768, c.vL, c.ldsw);
    else if constexpr (SG == 79) glds16<IMM>(c.gW2 + 192 * 896 + 768, c.vLd, c.ldsw);
    else glds16<IMM>(c.gWo + 768, c.vLo, c.ldsw);
}
template <int SG, int VM, int LG> DEV void sync_raw(const Ctx& c) {
    SBAR();
    asm volatile("s_waitcnt vmcnt(%0) lgkmcnt(%1)\n\ts_barrier" :: "n"(VM), "n"(LG) : "memory");
    if constexpr (SG >= 2 && SG + 10 < NSLAB) issue<SG + 10>(c);
    if constexpr (SG >= 2 && SG + 11 < NSLAB) issue<SG + 11>(c);
    SBAR();
}
constexpr u32 slot_off(int sg) { return (u32)((sg % RING) * SLABB); }
}

#define STAMP(i) do { if (lane == 0) { SBAR(); ((GAS u64*)(ws + W_TS))[((size_t)blockIdx.x * 8 + w) * 16 + (i)] = __builtin_amdgcn_s_memrealtime(); SBAR(); } } while (0)
#define STAMPR(i) do { SBAR(); asm volatile("s_memrealtime %0\n\ts_waitcnt lgkmcnt(0)" : "=s"(tsv[i]) :: "memory"); SBAR(); } while (0)

__global__ __launch_bounds__(512, 2) void fused_block(const u16* __restrict__ x, const u16* __restrict__ wqkv, const u16* __restrict__ wo,
                                                      const u16* __restrict__ w1, const u16* __restrict__ b1, const u16* __restrict__ w2,
                                                      const u16* __restrict__ b2, u16* __restrict__ out, char* __restrict__ ws) {
    constexpr u32 L0 = 0;
    u64 tfirst; asm volatile("s_memrealtime %0\n\ts_waitcnt lgkmcnt(0)" : "=s"(tfirst) :: "memory"); SBAR();
    asm volatile("" :: "s"(x), "s"(wqkv), "s"(wo), "s"(w1), "s"(b1), "s"(w2), "s"(b2), "s"(out), "s"(ws));
    const int lane = threadIdx.x & 63;
    const int w = __builtin_amdgcn_readfirstlane(threadIdx.x >> 6);
    const int th = w & 1, g = w >> 1;
    const int xcd = blockIdx.x & 7, jj = blockIdx.x >> 3;
    const int s = (xcd % (8 / BSL)) * BSL + (jj % BSL);
    const int t = (xcd / (8 / BSL)) * (32 / BSL) + jj / BSL;
    u64 tsv[16];
    for (int i = 0; i < 16; ++i) tsv[i] = 0;
    STAMPR(0);
    tsv[13] = tfirst;
    u64 cy0; asm volatile("s_memtime %0\n\ts_waitcnt lgkmcnt(0)" : "=s"(cy0) :: "memory");

    u32x4 xr[7];
    const int xrow = 8 * w + (lane >> 3);
    const int rot = (jj % BSL) * (8 / BSL);
    {
        const char* xb = (const char*)x + (size_t)t * (64 * 896);
        const u32 vx = xrow * 896 + (lane & 7) * 16;
#pragma unroll
        for (int j = 0; j < 7; ++j) {
            int jr = j + rot; jr = jr >= 7 ? jr - 7 : jr;
            asm volatile("global_load_dwordx4 %0, %1, %2" : "=v"(xr[j]) : "v"(vx), "s"(xb + jr * 128));
        }
    }
    Ctx c;
    {
        const int r = lane >> 4, ch = (lane & 15) ^ (r << 2), seg = ch >> 3, c8 = ch & 7, row = 4 * w + r;
        const int c7 = c8 > 6 ? 6 : c8;
        c.vKV = row * 2688 + seg * 896 + c7 * 16;
        c.vQ = (row + seg * 224) * 2688 + c7 * 16;
        c.vW1 = row * 3584 + ch * 16;
        c.vW2 = row * 896 + ch * 16;
        c.vWo1 = (row + 32 > 55 ? 55 : row + 32) * 896 + ch * 16;
        c.vL = (row + 32 * seg) * 896 + c8 * 16;
        c.vLd = row * 896 + c8 * 16;
        c.vLo = (row + 32 * seg > 55 ? 55 : row + 32 * seg) * 896 + c8 * 16;
        c.gKV = (const char*)wqkv + 896 + 112 * s;
        c.gQ = (const char*)wqkv + 112 * s;
        const int wb = 448 * s - 64 * (s & 1);
        c.gW1a = (const char*)w1 + wb + ((s & 1) ? 256 : 0);
        c.gW1b = (const char*)w1 + wb + ((s & 1) ? 0 : 256);
        c.gW2 = (const char*)w2 + (size_t)s * (224 * 896);
        c.gWo = (const char*)wo + (size_t)s * (56 * 896);
        c.ldsw = L0 + w * 1024;
    }
    constexpr int NPF = 2;
    {
        const int s0 = s - (jj % BSL);
        const int tid = threadIdx.x;
        {
            constexpr int LPS = (BSL == 1) ? 2 : (BSL == 2 ? 3 : 4);
            const int base = (112 * s0) & ~127;
            int i = tid; const int n = 14 * 3 * LPS;
            u32 off;
            const char* sb = (const char*)wqkv;
            i = i < n ? i : n - 1;
            const int m = i / (14 * LPS), rem = i % (14 * LPS), rr = rem / LPS, ll = rem % LPS;
            const int mm = m == 2 ? 0 : m + 1;
            int col = base + ll * 128; col = col > 768 ? 768 : col;
            off = (14 * jj + rr) * 2688 + mm * 896 + col;
            if (w * 64 < n) touch(sb, off, L0 + L_DUMMY);
            else {
                constexpr int LW = (BSL == 1) ? 4 : (7 * BSL / 2);
                constexpr int W0 = (14 * 3 * LPS + 63) / 64;
                int k = tid - W0 * 64; const int n1 = 14 * LW;
                k = k < n1 ? k : n1 - 1;
                const int bw = (448 * s0) & ~127;
                touch((const char*)w1, (14 * jj + k / LW) * 3584 + bw + (k % LW) * 128, L0 + L_DUMMY);
            }
        }
        {
            constexpr int N2 = 49 * BSL, NO = (392 * BSL + 31) / 32;
            if (w == 7) {
                const int wb = 448 * s - 64 * (s & 1);
                glds16<L_B1>((const char*)b1 + wb, (lane & 31) * 16, L0);
            } else if (w >= 5) {
                int k = jj * NO + (tid - 320 < NO ? tid - 320 : NO - 1); k = k < 392 * BSL ? k : 392 * BSL - 1;
                touch((const char*)wo + (size_t)s0 * (56 * 896), k * 128, L0 + L_DUMMY);
            } else {
                int k = tid < N2 ? tid : N2 - 1;
                touch((const char*)w2 + (size_t)s0 * (224 * 896), (jj * N2 + k) * 128, L0 + L_DUMMY);
            }
        }
    }
    issue<0>(c); issue<1>(c); issue<2>(c); issue<3>(c); issue<4>(c); issue<5>(c);
    issue<6>(c); issue<7>(c); issue<8>(c); issue<9>(c); issue<10>(c); issue<11>(c);
    u64 epoch;
    asm volatile("s_load_dwordx2 %0, %1, 0x0" : "=s"(epoch) : "s"(ws));

    asm volatile("s_waitcnt vmcnt(%8) lgkmcnt(0)" : "+v"(xr[0]), "+v"(xr[1]), "+v"(xr[2]), "+v"(xr[3]), "+v"(xr[4]), "+v"(xr[5]), "+v"(xr[6]), "+s"(epoch) : "n"(12 + NPF));
    STAMPR(1);
    const u64 tag64 = epoch + 0x100000001ull;
    const u32 tag = (u32)tag64;
    {
        float s1 = 0.f, s2 = 0.f, s1b = 0.f, s2b = 0.f;
#pragma unroll
        for (int j = 0; j < 7; ++j)
#pragma unroll
            for (int e = 0; e < 4; ++e) {
                float a = bflo(xr[j][e]), b = bfhi(xr[j][e]);
                s1 += a; s2 = __builtin_fmaf(a, a, s2); s1b += b; s2b = __builtin_fmaf(b, b, s2b);
            }
        s1 += s1b; s2 += s2b;
        s1 += dpp<0xB1>(s1); s2 += dpp<0xB1>(s2);
        s1 += dpp<0x4E>(s1); s2 += dpp<0x4E>(s2);
        s1 += dpp<0x141>(s1); s2 += dpp<0x141>(s2);
        const float mean = s1 * (1.f / DM);
        const float var = __builtin_fmaxf(__builtin_fmaf(-mean, mean, s2 * (1.f / DM)), 0.f);
        const float rstd = __builtin_amdgcn_rsqf(var + 1e-5f);
        const float nb = -mean * rstd;
        const u32 xa = L0 + L_XN + xrow * XS + (lane & 7) * 16;
#pragma unroll
        for (int j = 0; j < 7; ++j) {
            int jr = j + rot; jr = jr >= 7 ? jr - 7 : jr;
            u32x4 o;
#pragma unroll
            for (int e = 0; e < 4; ++e) o[e] = cvtpk(__builtin_fmaf(bflo(xr[j][e]), rstd, nb), __builtin_fmaf(bfhi(xr[j][e]), rstd, nb));
            lds_st<u32x4>(xa + jr * 128, o);
        }
        if ((lane & 7) == 0) lds_st<float>(L0 + L_R2 + xrow * 4, __builtin_amdgcn_rsqf(var * rstd * rstd + 1e-5f));
    }
    asm volatile("s_waitcnt lgkmcnt(0)\n\ts_barrier" ::: "memory");
    const int l31 = lane & 31, hf = lane >> 5;
    bf16x8 A[28];
    {
        const u32 a = L0 + L_XN + (32 * th + l31) * XS + hf * 16;
#pragma unroll
        for (int j = 0; j < 28; ++j) A[j] = lds_ld<bf16x8>(a + j * 32);
    }
    const float r2 = lds_ld<float>(L0 + L_R2 + (32 * th + l31) * 4);

    const int blk = (lane >> 4) & 1, q = (lane & 15) >> 2, p = lane & 3;
    const u32 trrow = L0 + (8 * hf + q) * 256;
    const u32 mstd = 32 * blk + 8 * p, mperm = 32 * (p & 1) + 16 * blk + 8 * (p >> 1);
    const u32 tKV = trrow + ((((g >> 1) * 128 + (g & 1) * 56) + mstd) ^ (q << 6));
    const int gq = (s & 1) ? 3 - g : g;
    const u32 tW1 = trrow + ((64 * gq + mperm) ^ (q << 6));
    const u32 tQ = trrow + (((g & 1) * 56 + mperm) ^ (q << 6));
    const u32 tO = trrow + ((64 * g + mperm) ^ (q << 6));
    const u32 hrow = L0 + L_H + (32 * th + l31) * HS;

    char* const wsP = ws + W_P + (size_t)s * (32 * 32 * 512);
    char* const wsR = ws + W_R + (size_t)s * 8192;
    char* const wsO = ws + W_O + (size_t)t * (8 * 7 * 8 * 1024);
    char* const wsF = ws + W_FL + (size_t)t * (8 * 256);

    bf16x8 Bc0, Bc1, Bn0, Bn1;
    f32x16 acc, accP;
    const f32x16 zero16 = {0.f, 0.f, 0.f, 0.f, 0.f, 0.f, 0.f, 0.f, 0.f, 0.f, 0.f, 0.f, 0.f, 0.f, 0.f, 0.f};

    auto hop1_issue = [&]() { const char* src = wsP + (size_t)t * 16384 + w * 2048; glds16c<L_KVX>(src, lane * 16, L0 + w * 2048); glds16c<L_KVX + 1024>(src + 1024, lane * 16, L0 + w * 2048); };
    auto hop1_check = [&]() {
        u32x4 a, b;
        for (u32 spin = 0;; ++spin) {
            a = lds_ld<u32x4>(L0 + L_KVX + w * 2048 + lane * 16); b = lds_ld<u32x4>(L0 + L_KVX + w * 2048 + 1024 + lane * 16);
            const bool ok = a[1] == tag && a[3] == tag && b[1] == tag && b[3] == tag;
            if (__all(ok) || spin >= SPIN_CAP) break;
            hop1_issue(); waitvm<0>();
        }
        float v0 = __uint_as_float(a[0]) + __uint_as_float(b[0]), v1 = __uint_as_float(a[2]) + __uint_as_float(b[2]);
        auto r0 = __builtin_amdgcn_permlane32_swap(__float_as_uint(v0), __float_as_uint(v0), false, false);
        auto r1 = __builtin_amdgcn_permlane32_swap(__float_as_uint(v1), __float_as_uint(v1), false, false);
        f32x2 o = {__uint_as_float(r0[0]) + __uint_as_float(r0[1]), __uint_as_float(r1[0]) + __uint_as_float(r1[1])};
        if (lane < 32) lds_st<f32x2>(L0 + L_RED + w * 256 + lane * 8, o);
    };
    auto hop1_publish = [&]() {
        if (w == 6) {
            f32x2 v = lds_ld<f32x2>(L0 + L_RED + l31 * 8);
#pragma unroll
            for (int i = 1; i < 8; ++i) v += lds_ld<f32x2>(L0 + L_RED + i * 256 + l31 * 8);
            if (lane < 32) st8c(wsR + t * 256, lane * 8, (u32x2){cvtpk(v[0], v[1]), tag});
        }
    };
    auto hop2_issue = [&]() { glds16c<L_KVX>(wsR + w * 1024, lane * 16, L0 + w * 2048); };
    auto hop2_check = [&]() {
        u32x4 a;
        for (u32 spin = 0;; ++spin) {
            a = lds_ld<u32x4>(L0 + L_KVX + w * 2048 + lane * 16);
            const bool ok = a[1] == tag && a[3] == tag;
            if (__all(ok) || spin >= SPIN_CAP) break;
            hop2_issue(); waitvm<0>();
        }
        const int Ln = 32 * ((lane >> 4) & 1) + 2 * (lane & 15);
        const u32 ad = L0 + L_KVB + (w >> 2) * 2560 + (16 * (w & 1) + (Ln & 15)) * 80 + (16 * ((w >> 1) & 1) + 4 * (Ln >> 4) + 2 * hf) * 2;
        lds_st<u32>(ad, a[0]); lds_st<u32>(ad + 80, a[2]);
    };
#define SYNC(SG, LG) do { \
        constexpr bool full_ = ((SG) == 16 || (SG) == H1C + 2 || (SG) == 48 || (SG) == H2C + 2 || (SG) == H2C + 4); \
        constexpr int vmr_ = vmwait(SG); \
        constexpr int vm1_ = (SG) == H1C ? younger_than_extra(H1I, SG) : ((SG) == H2C ? younger_than_extra(H2I, SG) : ((SG) == FL01 ? younger_than_extra(66, SG) : 63)); \
        sync_raw<SG, (vm1_ < vmr_ ? vm1_ : vmr_), (full_ ? 0 : (LG))>(c); \
        if constexpr ((SG) == H1I) hop1_issue(); \
        if constexpr ((SG) == H2I) hop2_issue(); \
        if constexpr ((SG) == H1C) { hop1_check(); STAMPR(5); } \
        if constexpr ((SG) == H1C + 2) hop1_publish(); \
        if constexpr ((SG) == H2C) { hop2_check(); STAMPR(7); } \
    } while (0)
#define RD(SG, T) do { Bn0 = trfrag((T) + slot_off(SG)); Bn1 = trfrag((T) + slot_off(SG) + 4096); } while (0)
#define ROT() do { Bc0 = Bn0; Bc1 = Bn1; } while (0)

    u32 hp[8];
    u32x4 bq0, bq1;
    auto gelu2 = [&](int i) {
        const u32 bb = i < 4 ? bq0[i & 3] : bq1[i & 3];
        hp[i] = cvtpk(gelu(__builtin_fmaf(accP[2 * i], r2, bflo(bb))), gelu(__builtin_fmaf(accP[2 * i + 1], r2, bfhi(bb))));
    };
    auto bias_ld = [&](int wblk) { const u32 a = L0 + L_B1 + wblk * 64 + hf * 32; bq0 = lds_ld<u32x4>(a); bq1 = lds_ld<u32x4>(a + 16); };
    auto h_st = [&](int wblk, int half, bool live) {
        const u32 a = live ? hrow + (wblk - (s & 1)) * 64 + hf * 32 + half * 16 : L0 + L_DUMMY;
        lds_st<u32x4>(a, (u32x4){hp[4 * half], hp[4 * half + 1], hp[4 * half + 2], hp[4 * half + 3]});
    };

    SYNC(0, 0);
    STAMPR(2);
    RD(0, tKV); ROT();
    acc = zero16;
#define G1STEP(SG) do { if constexpr ((SG) % 2 == 0 && (SG) > 0) SYNC(SG, 4); \
        if constexpr ((SG) < 13) RD((SG) + 1, tKV); else RD((SG) + 1, tW1); \
        acc = MFMA32(A[2 * (SG)], Bc0, acc); acc = MFMA32(A[2 * (SG) + 1], Bc1, acc); ROT(); } while (0)
    G1STEP(0); G1STEP(1); G1STEP(2); G1STEP(3); G1STEP(4); G1STEP(5); G1STEP(6);
    G1STEP(7); G1STEP(8); G1STEP(9); G1STEP(10); G1STEP(11); G1STEP(12); G1STEP(13);
    accP = acc; acc = zero16;
    STAMPR(3);

#define W1STEP(SG, B0, TN) do { RD((SG) + 1, TN); \
        acc = MFMA32(Bc0, A[2 * ((SG) - (B0))], acc); acc = MFMA32(Bc1, A[2 * ((SG) - (B0)) + 1], acc); ROT(); } while (0)
    SYNC(14, 4);
    W1STEP(14, 14, tW1);
    {
        u32 pk[8];
        if (g < 2) {
#pragma unroll
            for (int i = 0; i < 8; ++i) { u32 v = cvtpk(phi(accP[2 * i]), phi(accP[2 * i + 1])); pk[i] = l31 < 28 ? v : 0u; }
        } else {
#pragma unroll
            for (int i = 0; i < 8; ++i) { u32 v = cvtpk(accP[2 * i], accP[2 * i + 1]); pk[i] = l31 == 28 ? 0x3f803f80u : v; }
        }
        const u32 a = L0 + L_KVX + (g * 8 + th * 4 + hf * 2) * 512 + l31 * 16;
        lds_st<u32x4>(a, (u32x4){pk[0], pk[1], pk[2], pk[3]});
        lds_st<u32x4>(a + 512, (u32x4){pk[4], pk[5], pk[6], pk[7]});
        if (w == 7) lds_st<u32x4>(L0 + L_H + lane * HS + 560, (u32x4){0u, 0u, 0u, 0u});
    }
    W1STEP(15, 14, tW1);
    SYNC(16, 4);
    {
        const u32 ka = L0 + L_KVX + ((w >> 2) * 8 + (lane >> 4)) * 512 + (16 * ((w >> 1) & 1) + (lane & 15)) * 16;
        const u32 va = L0 + L_KVX + ((2 + (w >> 2)) * 8 + (lane >> 4)) * 512 + (16 * (w & 1) + (lane & 15)) * 16;
        f32x4 kv = {0.f, 0.f, 0.f, 0.f};
        kv = MFMA16(lds_ld<bf16x8>(ka), lds_ld<bf16x8>(va), kv);
        kv = MFMA16(lds_ld<bf16x8>(ka + 2048), lds_ld<bf16x8>(va + 2048), kv);
        const u32 vo = ((4 * w + hf) * 32 + t) * 512 + l31 * 16;
        st16c(wsP, vo, (u32x4){__float_as_uint(kv[0]), tag, __float_as_uint(kv[1]), tag});
        st16c(wsP + 2 * 16384, vo, (u32x4){__float_as_uint(kv[2]), tag, __float_as_uint(kv[3]), tag});
    }
    STAMPR(4);
    W1STEP(16, 14, tW1); W1STEP(17, 14, tW1);
#define W1PAIR(SG, B0) do { SYNC(SG, 4); W1STEP(SG, B0, tW1); W1STEP((SG) + 1, B0, tW1); } while (0)
    W1PAIR(18, 14); W1PAIR(20, 14); W1PAIR(22, 14); W1PAIR(24, 14); W1PAIR(26, 14);
    accP = acc; acc = zero16;

    const int wbA = (s & 1) ? 7 - g : g, wbB = (s & 1) ? 3 - g : 4 + g;
    SYNC(28, 4); bias_ld(wbA); W1STEP(28, 28, tW1); W1STEP(29, 28, tW1); gelu2(0);
    SYNC(30, 4); W1STEP(30, 28, tW1); gelu2(1); W1STEP(31, 28, tW1);
    SYNC(32, 4); W1STEP(32, 28, tW1); gelu2(2); W1STEP(33, 28, tW1); gelu2(3);
    SYNC(34, 4); h_st(wbA, 0, true); W1STEP(34, 28, tW1); gelu2(4); W1STEP(35, 28, tW1);
    SYNC(36, 4); W1STEP(36, 28, tW1); gelu2(5); W1STEP(37, 28, tW1);
    SYNC(38, 4); W1STEP(38, 28, tW1); gelu2(6); W1STEP(39, 28, tW1);
    SYNC(40, 4); W1STEP(40, 28, tW1); gelu2(7); h_st(wbA, 1, true); W1STEP(41, 28, tQ);
    accP = acc; acc = zero16;
    STAMPR(6);

    bf16x8 Hf[18];
    u32 pq[8];
    if (g < 2) {
        SYNC(42, 4); bias_ld(wbB); gelu2(0); gelu2(1); gelu2(2);
        SYNC(44, 4); gelu2(3); h_st(wbB, 0, true); gelu2(4); gelu2(5);
        SYNC(46, 4); gelu2(6); gelu2(7); h_st(wbB, 1, true);
        SYNC(48, 4);
        RD(49, tO); ROT();
#pragma unroll
        for (int j = 0; j < 14; ++j) Hf[j] = lds_ld<bf16x8>(hrow + hf * 16 + j * 32);
#pragma unroll
        for (int i = 0; i < 8; ++i) pq[i] = 0;
    } else {
        const u32 tQ2 = tQ ^ 128u;
#define QSTEP(SG, TN) do { RD(SG, tQ2); \
        acc = MFMA32(Bc0, A[2 * ((SG) - 42)], acc); acc = MFMA32(Bc1, A[2 * ((SG) - 42) + 1], acc); ROT(); \
        RD((SG) + 1, TN); \
        acc = MFMA32(Bc0, A[14 + 2 * ((SG) - 42)], acc); acc = MFMA32(Bc1, A[15 + 2 * ((SG) - 42)], acc); ROT(); } while (0)
        SYNC(42, 4); bias_ld(wbB); QSTEP(42, tQ); gelu2(0); QSTEP(43, tQ); gelu2(1); gelu2(2);
        SYNC(44, 4); QSTEP(44, tQ); gelu2(3); h_st(wbB, 0, g == 2); gelu2(4); QSTEP(45, tQ); gelu2(5);
        SYNC(46, 4); QSTEP(46, tQ); gelu2(6); gelu2(7); h_st(wbB, 1, g == 2); QSTEP(47, tQ);
        SYNC(48, 4);
        QSTEP(48, tO);
#pragma unroll
        for (int j = 0; j < 14; ++j) Hf[j] = lds_ld<bf16x8>(hrow + hf * 16 + j * 32);
#pragma unroll
        for (int i = 0; i < 8; ++i) pq[i] = cvtpk(phi(acc[2 * i]), phi(acc[2 * i + 1]));
    }
    acc = zero16; accP = zero16;

    auto attn_o = [&]() {
        if (g >= 2) {
            const int m = l31, e = 16 * ((m >> 2) & 1) + 4 * (m >> 3) + (m & 3);
            const u32 ka = L0 + L_KVB + (g & 1) * 2560 + e * 80 + hf * 32;
            f32x16 nm = MFMA32(lds_ld<bf16x8>(ka), __builtin_bit_cast(bf16x8, (u32x4){pq[0], pq[1], pq[2], pq[3]}), zero16);
            nm = MFMA32(lds_ld<bf16x8>(ka + 16), __builtin_bit_cast(bf16x8, (u32x4){pq[4], pq[5], pq[6], pq[7]}), nm);
            auto dd = __builtin_amdgcn_permlane32_swap(__float_as_uint(nm[12]), __float_as_uint(nm[12]), false, false);
            const float rd = __builtin_amdgcn_rcpf(__uint_as_float(dd[1]) + 1e-6f);
            const u32 oa = hrow + 448 + (g & 1) * 56 + hf * 32;
#pragma unroll
            for (int i = 0; i < 4; ++i) {
                u32x2 v = {cvtpk(nm[4 * i] * rd, nm[4 * i + 1] * rd), cvtpk(nm[4 * i + 2] * rd, nm[4 * i + 3] * rd)};
                if (i < 3 || hf == 0) lds_st<u32x2>(oa + i * 8, v);
            }
        }
    };
    auto tile_st = [&](const f32x16& a, char* p0, char* p1) {
        st16c(p0, lane * 16, (u32x4){cvtpk(a[0], a[1]), cvtpk(a[2], a[3]), cvtpk(a[4], a[5]), cvtpk(a[6], a[7])});
        st16c(p1, lane * 16, (u32x4){cvtpk(a[8], a[9]), cvtpk(a[10], a[11]), cvtpk(a[12], a[13]), cvtpk(a[14], a[15])});
    };
    auto flag_st = [&](int dest, int j) { if (lane == 0) st8c(wsF + dest * 256 + j * 64 + s * 8, 0u, (u32x2){tag, (u32)(tag64 >> 32)}); };
    char* const otile = wsO + (size_t)(w * 7 * 8 + s) * 1024;
#define OSTEP(SG, K0, ACC) do { \
        if constexpr ((SG) % 2 == 0) { SYNC(SG, 4); \
            if constexpr ((SG) == H2C + 2) attn_o(); \
            if constexpr ((SG) == H2C + 4) { _Pragma("unroll") for (int j = 14; j < 18; ++j) Hf[j] = lds_ld<bf16x8>(hrow + hf * 16 + j * 32); } \
            if constexpr ((SG) == FL01) { flag_st(w, 0); flag_st(w, 1); } } \
        RD((SG) + 1, tO); \
        ACC = MFMA32(Bc0, Hf[K0], ACC); ACC = MFMA32(Bc1, Hf[(K0) + 1], ACC); ROT(); } while (0)
    static_assert(H2C + 4 <= 62 && H2C >= 50 && H2I >= H1C + 4 && H1I >= 18, "schedule");
    OSTEP(49, 0, acc); OSTEP(50, 2, acc); OSTEP(51, 4, acc); OSTEP(52, 6, acc); OSTEP(53, 8, acc); OSTEP(54, 10, acc); OSTEP(55, 12, acc);
    OSTEP(56, 0, accP); OSTEP(57, 2, accP); OSTEP(58, 4, accP); OSTEP(59, 6, accP); OSTEP(60, 8, accP); OSTEP(61, 10, accP); OSTEP(62, 12, accP);
    STAMPR(8);
    OSTEP(63, 14, acc); OSTEP(64, 16, acc);
    OSTEP(65, 14, accP);
    tile_st(acc, otile, otile + 8192); acc = zero16;
    OSTEP(66, 16, accP);
    OSTEP(67, 0, acc);
    tile_st(accP, otile + 2 * 8192, otile + 3 * 8192);
    OSTEP(68, 2, acc); OSTEP(69, 4, acc); OSTEP(70, 6, acc); OSTEP(71, 8, acc); OSTEP(72, 10, acc); OSTEP(73, 12, acc);
    OSTEP(74, 14, acc); OSTEP(75, 16, acc);
    sync_raw<76, (FL01 == 74 ? 2 : 0), 0>(c);
    STAMPR(9);
    tile_st(acc, otile + 4 * 8192, otile + 5 * 8192);

    auto finish_unit = [&](int u) {
        const int j = u >> 1;
        int tk, col;
        if (u < 6) { tk = 32 * (s & 1) + l31; col = 128 * j + 32 * (s >> 1) + 16 * hf + 8 * (u & 1); }
        else { tk = 32 * ((s >> 1) & 1) + l31; col = 384 + 32 * (s >> 2) + 16 * hf + 8 * (s & 1); }
        const size_t eo = (size_t)(64 * t + tk) * DM + col;
        const u32x4 xv = *(const GAS u32x4*)(x + eo);
        const u32x4 bv = *(const GAS u32x4*)(b2 + col);
        const GAS u64* fl = (const GAS u64*)(wsF + s * 256 + j * 64) + (lane & 7);
        for (u32 spin = 0; spin < SPIN_CAP; ++spin) {
            const u64 f = __hip_atomic_load(fl, __ATOMIC_RELAXED, __HIP_MEMORY_SCOPE_AGENT);
            if (__all(f == tag64)) break;
        }
        const char* src = wsO + (size_t)((s * 7 + u) * 8) * 1024;
        u32x4 pv[8];
        const u32 vo = lane * 16;
        asm volatile("global_load_dwordx4 %0, %8, %9 sc1\n\tglobal_load_dwordx4 %1, %8, %9 offset:1024 sc1\n\t"
                     "global_load_dwordx4 %2, %8, %9 offset:2048 sc1\n\tglobal_load_dwordx4 %3, %8, %9 offset:3072 sc1\n\t"
                     "global_load_dwordx4 %4, %8, %10 sc1\n\tglobal_load_dwordx4 %5, %8, %10 offset:1024 sc1\n\t"
                     "global_load_dwordx4 %6, %8, %10 offset:2048 sc1\n\tglobal_load_dwordx4 %7, %8, %10 offset:3072 sc1\n\t"
                     "s_waitcnt vmcnt(0)"
                     : "=&v"(pv[0]), "=&v"(pv[1]), "=&v"(pv[2]), "=&v"(pv[3]), "=&v"(pv[4]), "=&v"(pv[5]), "=&v"(pv[6]), "=&v"(pv[7])
                     : "v"(vo), "s"(src), "s"(src + 4096) : "memory");
        u32x4 ov;
#pragma unroll
        for (int e = 0; e < 4; ++e) {
            float lo = bflo(pv[0][e]), hi = bfhi(pv[0][e]);
#pragma unroll
            for (int i = 1; i < 8; ++i) { lo += bflo(pv[i][e]); hi += bfhi(pv[i][e]); }
            ov[e] = cvtpk(lo + bflo(xv[e]) + bflo(bv[e]), hi + bfhi(xv[e]) + bfhi(bv[e]));
        }
        *(GAS u32x4*)(out + eo) = ov;
    };

    if (g < 2) {
        const u32 tO2 = tO ^ 128u;
        acc = zero16;
#define LSTEP(SG, K0) do { RD(SG, tO2); \
        acc = MFMA32(Bc0, Hf[K0], acc); acc = MFMA32(Bc1, Hf[(K0) + 1], acc); ROT(); \
        if constexpr ((SG) < 80) RD((SG) + 1, tO); \
        acc = MFMA32(Bc0, Hf[(K0) + 2], acc); acc = MFMA32(Bc1, Hf[(K0) + 3], acc); ROT(); } while (0)
        LSTEP(76, 0); LSTEP(77, 4); LSTEP(78, 8);
        RD(80, tO); acc = MFMA32(Bc0, Hf[12], acc); acc = MFMA32(Bc1, Hf[13], acc); ROT();
        LSTEP(80, 14);
        char* o6 = wsO + (size_t)(((2 * w) * 7 + 6) * 8 + s) * 1024;
        tile_st(acc, o6, o6 + 7 * 8192);
        STAMPR(10);
        waitvm<2>(); flag_st(w, 2);
        waitvm<1>(); flag_st(2 * w, 3); flag_st(2 * w + 1, 3);
        STAMPR(11);
    } else {
        waitvm<0>(); flag_st(w, 2);
        STAMPR(10);
        finish_unit(w - 4);
        STAMPR(11);
        if (w < 7) finish_unit(w);
        else if (blockIdx.x == 0 && lane == 0) st8c(ws, 0u, (u32x2){tag, (u32)(tag64 >> 32)});
    }
    STAMPR(12);
    { u64 cy1; asm volatile("s_memtime %0\n\ts_waitcnt lgkmcnt(0)" : "=s"(cy1) :: "memory"); tsv[15] = cy1 - cy0; }
    if (lane == 0) for (int i = 0; i < 16; ++i) ((GAS u64*)(ws + W_TS))[((size_t)blockIdx.x * 8 + w) * 16 + i] = tsv[i];
}

__global__ void k_pre(u64* ts) { ts[2048 * 16] = __builtin_amdgcn_s_memrealtime(); }
__global__ void k_report(const u64* ts) {
    const u64 tme = __builtin_amdgcn_s_memrealtime();
    const int l = threadIdx.x;
    u64 t0 = ~0ull;
    for (int w = l; w < 2048; w += 64) { u64 v = ts[w * 16]; t0 = v < t0 ? v : t0; }
    for (int o = 32; o; o >>= 1) { u64 v = __shfl_xor(t0, o); t0 = v < t0 ? v : t0; }
    float av[13], mx[13];
#pragma unroll
    for (int i = 0; i < 13; ++i) {
        float s = 0.f, m = 0.f;
        for (int w = l; w < 2048; w += 64) { float d = (float)(ts[w * 16 + i] - t0) * 0.01f; s += d; m = d > m ? d : m; }
        for (int o = 32; o; o >>= 1) { s += __shfl_xor(s, o); float v = __shfl_xor(m, o); m = v > m ? v : m; }
        av[i] = s / 2048.f; mx[i] = m;
    }
    float ck = (float)ts[15] / ((float)(ts[12] - ts[0]) * 10.f);
    float v = WHICH == 0 ? ck * 5.f : (WHICH == 1 ? mx[0] : (WHICH < 20 ? av[(WHICH + 19) % 20 % 13] : mx[(WHICH + 19) % 20 % 13]));
    if (WHICH == 40) v = (float)(tme - t0) * 0.01f - mx[12];
    if (WHICH == 41) v = (float)(t0 - ts[2048 * 16]) * 0.01f;
    if (WHICH == 42 || WHICH == 45) v = mx[12] * 0.001f;
    if (WHICH == 43 || WHICH == 44) { u64 t1 = ~0ull; for (int w = l; w < 2048; w += 64) { u64 q = ts[w * 16 + 13]; t1 = q < t1 ? q : t1; }
        for (int o = 32; o; o >>= 1) { u64 q = __shfl_xor(t1, o); t1 = q < t1 ? q : t1; }
        v = WHICH == 43 ? (float)(t0 - t1) * 0.01f : (float)(t1 - ts[2048 * 16]) * 0.01f; }
    u64 c0 = __builtin_amdgcn_s_memrealtime(); u64 dt = (u64)(v * 2000.f); dt = dt > 200000 ? 200000 : dt;
    for (int i = 0; i < 1000000; ++i) { if (__builtin_amdgcn_s_memrealtime() - c0 >= dt) break; __builtin_amdgcn_s_sleep(1); }
}
extern "C" void kernel_launch(void* const* d_in, const int* in_sizes, int n_in, void* d_out, int out_size, void* d_ws, size_t ws_size,
                              hipStream_t stream) {
    (void)in_sizes; (void)n_in; (void)out_size; (void)ws_size;
    (void)hipFuncSetAttribute((const void*)fused_block, hipFuncAttributeMaxDynamicSharedMemorySize, LDS_BYTES);
    hipLaunchKernelGGL(k_pre, dim3(1), dim3(64), 0, stream, (u64*)((char*)d_ws + W_TS));
    hipLaunchKernelGGL(fused_block, dim3(256), dim3(512), LDS_BYTES, stream,
                       (const u16*)d_in[0], (const u16*)d_in[1], (const u16*)d_in[2], (const u16*)d_in[4], (const u16*)d_in[5],
                       (const u16*)d_in[6], (const u16*)d_in[7], (u16*)d_out, (char*)d_ws);
    hipLaunchKernelGGL(k_report, dim3(1), dim3(64), 0, stream, (const u64*)((char*)d_ws + W_TS));
}
```

```cpp
#pragma clang diagnostic ignored "-Wint-to-pointer-cast"
#include <hip/hip_runtime.h>
#include <stdint.h>

typedef unsigned short u16;
typedef unsigned u32;
typedef unsigned long long u64;
using bf16x8 = __attribute__((ext_vector_type(8))) short;
using s16x4 = __attribute__((ext_vector_type(4))) short;
using f32x16 = __attribute__((ext_vector_type(16))) float;
using f32x4 = __attribute__((ext_vector_type(4))) float;
using f32x2 = __attribute__((ext_vector_type(2))) float;
using u32x4 = __attribute__((ext_vector_type(4))) unsigned;
using u32x2 = __attribute__((ext_vector_type(2))) unsigned;
#define LAS __attribute__((address_space(3)))
#define GAS __attribute__((address_space(1)))
#define DEV __device__ __forceinline__
#define INL __attribute__((always_inline))

#define BSL 2
#define PD 2
#define H1I 17
#define H1C 20
#define H2I 24
#define H2C 27
#define PADN 2
#define LEAD0 12
#define RS0 11
#define RAMP 2
#define GSCH 2, 2, 2, 2, 2, 0, 2
#define NPL 6
#define TME 1
#define TML 12
#define TMA 2
#define LEADA 8
#define NA 5
#define SGP 5
#define KST (8 * (KES + KSP) + 11)
#define GE 183
#define KES 10
#define XST 33
#define KSP 1
#define HSP 1
#define PS0 12
#define FST 64
#define TLT 41
#define LASTB 38

namespace {
constexpr int DM = 448;
constexpr int NBLK = 161, NST = 41, RING = 6, STB = 16384;
constexpr int L_XN = RING * STB;
constexpr int XS = 912, HS = 592;
constexpr int L_H = L_XN;
constexpr int L_KVS = L_XN + 64 * HS;
constexpr int L_KVG = L_H;
constexpr int L_MISC = L_XN + 64 * XS;
constexpr int L_IR = L_MISC;
constexpr int L_B1 = L_MISC + 256;
constexpr int L_KVB = L_B1 + 1024;
constexpr int L_DUMMY = L_KVB + 5120;
constexpr int L_ZERO = L_DUMMY + 256;
constexpr int LDS_BYTES = L_ZERO + 16;
static_assert(L_KVS + 16384 <= L_MISC, "lds");
static_assert(LDS_BYTES <= 163840, "lds");
constexpr size_t W_FL = 4096, W_P = 0x20000 + 1024 * FST, W_R = W_P + (2u << 20), W_O = W_R + 65536;
constexpr size_t W_TS = 64u << 20;
constexpr u32 SPIN_CAP = 1u << 18;

template <int V> struct IC { static constexpr int value = V; };
template <int I, int N, typename F> DEV void sfor(F&& f) { if constexpr (I < N) { f(IC<I>{}); sfor<I + 1, N>(f); } }

typedef __bf16 bf16x2_t __attribute__((ext_vector_type(2)));
DEV u32 cvtpk(float lo, float hi) { f32x2 v = {lo, hi}; return __builtin_bit_cast(u32, __builtin_convertvector(v, bf16x2_t)); }
DEV float bflo(u32 v) { return __uint_as_float(v << 16); }
DEV float bfhi(u32 v) { return __uint_as_float(v & 0xffff0000u); }
DEV float dot2(u32 a, u32 b, float c) { return __builtin_amdgcn_fdot2_f32_bf16(__builtin_bit_cast(bf16x2_t, a), __builtin_bit_cast(bf16x2_t, b), c, false); }
template <int CTRL> DEV float dpp(float v) { return __int_as_float(__builtin_amdgcn_update_dpp(0, __float_as_int(v), CTRL, 0xf, 0xf, true)); }
template <int IMM, int K> DEV void glds16(const void* base, u32 so, u32 voff, u32 ldsw) {
    asm volatile("s_add_u32 m0, %2, %3\n\ts_nop 0\n\tglobal_load_lds_dwordx4 %0, %1" :: "v"(voff), "s"((const char*)base + so + K), "s"(ldsw), "n"(IMM) : "memory", "scc");
}
DEV void touch(const char* sb, u32 voff) {
    asm volatile("s_mov_b32 m0, %2\n\ts_nop 0\n\tglobal_load_lds_dword %0, %1" :: "v"(voff), "s"(sb), "n"(L_DUMMY) : "memory");
}
DEV void st16c(char* sb, u32 voff, u32x4 v) { asm volatile("global_store_dwordx4 %0, %1, %2 sc1\n\ts_nop 1" :: "v"(voff), "v"(v), "s"(sb) : "memory"); }
template <int OFF> DEV void sto(char* sb, u32 voff, u32x4 v) {
    asm volatile("global_store_dwordx4 %0, %1, %2 offset:%3\n\ts_nop 1" :: "v"(voff), "v"(v), "s"(sb), "n"(OFF) : "memory");
}
DEV void st8c(char* sb, u32 voff, u32x2 v) { asm volatile("global_store_dwordx2 %0, %1, %2 sc1" :: "v"(voff), "v"(v), "s"(sb) : "memory"); }
template <int OFF> DEV void ld16c(u32x4& d, const char* sb, u32 voff) { asm volatile("global_load_dwordx4 %0, %1, %2 offset:%3 sc1" : "=v"(d) : "v"(voff), "s"(sb), "n"(OFF) : "memory"); }
template <int OFF> DEV void ld16(u32x4& d, const char* sb, u32 voff) { asm volatile("global_load_dwordx4 %0, %1, %2 offset:%3" : "=v"(d) : "v"(voff), "s"(sb), "n"(OFF) : "memory"); }
DEV void ld8c(u64& d, const char* sb, u32 voff) { asm volatile("global_load_dwordx2 %0, %1, %2 sc1" : "=v"(d) : "v"(voff), "s"(sb) : "memory"); }
DEV void wait8(u64& d) { asm volatile("s_waitcnt vmcnt(0)" : "+v"(d) :: "memory"); }
template <int N> DEV void waitvm() { asm volatile("s_waitcnt vmcnt(%0)" :: "n"(N) : "memory"); }
template <int N> DEV void waitvm2(u32x4& a, u32x4& b) { asm volatile("s_waitcnt vmcnt(%2)" : "+v"(a), "+v"(b) : "n"(N) : "memory"); }
DEV void sld8(u64& e, const char* p) { asm volatile("s_load_dwordx2 %0, %1, 0x0" : "=s"(e) : "s"(p) : "memory"); }
template <int N> DEV void waitx(u32x4* v, u64& e) {
    asm volatile("s_waitcnt vmcnt(%8) lgkmcnt(0)" : "+v"(v[0]), "+v"(v[1]), "+v"(v[2]), "+v"(v[3]), "+v"(v[4]), "+v"(v[5]), "+v"(v[6]), "+s"(e) : "n"(N));
}
template <int N> DEV void waitvm4(u32x4* a) { asm volatile("s_waitcnt vmcnt(%4)" : "+v"(a[0]), "+v"(a[1]), "+v"(a[2]), "+v"(a[3]) : "n"(N) : "memory"); }
template <int M> DEV void st8m(char* sb, u32 voff, u64 v) {
    asm volatile("s_mov_b64 exec, %3\n\tglobal_store_dwordx2 %0, %1, %2 sc1\n\ts_mov_b64 exec, -1" :: "v"(voff), "v"(v), "s"(sb), "n"(M) : "memory");
}
DEV void ld2m(u32x4& a, u32x4& b, const char* sb, u32 voff, u64 mask) {
    asm volatile("s_mov_b64 exec, %4\n\tglobal_load_dwordx4 %0, %2, %3 offset:2048 sc1\n\tglobal_load_dwordx4 %1, %2, %3 offset:3072 sc1\n\ts_mov_b64 exec, -1"
                 : "=&v"(a), "=&v"(b) : "v"(voff), "s"(sb), "s"(mask) : "memory");
}
template <typename T> DEV T lds_ld(u32 a) { return *(const LAS T*)(a); }
template <typename T> DEV void lds_st(u32 a, T v) { *(LAS T*)(a) = v; }
typedef short v4i16_t __attribute__((ext_vector_type(4)));
DEV s16x4 tr4(u32 a) { return __builtin_amdgcn_ds_read_tr16_b64_v4i16((LAS v4i16_t*)(a)); }
DEV bf16x8 trfrag(u32 a) { return __builtin_shufflevector(tr4(a), tr4(a + 1024), 0, 1, 2, 3, 4, 5, 6, 7); }
#define MFMA32(a, b, c) __builtin_amdgcn_mfma_f32_32x32x16_bf16(a, b, c, 0, 0, 0)
#define SBAR() __builtin_amdgcn_sched_barrier(0)
template <typename T> DEV void pin(T& v) { asm volatile("" : "+v"(v)); }
template <typename T> DEV void pins(T& v) { asm volatile("" : "+s"(v)); }
DEV bool wall(bool p) { return __builtin_amdgcn_ballot_w64(!p) == 0; }
#define MFMA16(a, b, c) __builtin_amdgcn_mfma_f32_16x16x32_bf16(a, b, c, 0, 0, 0)
DEV bf16x8 asfrag(u32 a, u32 b, u32 c, u32 d) { return __builtin_bit_cast(bf16x8, (u32x4){a, b, c, d}); }

enum { K_KV, K_WA, K_WB, K_Q, K_G };
struct Blk { int kind, k, grp; };
constexpr Blk blk(int b) {
    if (b < 28) return {K_KV, b, 0};
    if (b < 56) return {K_WA, b - 28, 0};
    if (b < 84) return {K_WB, b - 56, 0};
    if (b < 98) return {K_Q, b - 84, 0};
    int r = b - 98;
    if (r < 4 * NA) return {K_G, r % 4, r / 4};
    r -= 4 * NA;
    if (r < 5 * NA) return {K_G, 4 + r % 5, r / 5};
    r -= 5 * NA;
    return {K_G, r % 9, NA + r / 9};
}
constexpr int gend(int g) { return g < NA ? 102 + 4 * NA + 5 * g : 106 + 9 * g; }
constexpr u32 boff(int b) { return (u32)(((b >> 2) % RING) * STB + (b & 3) * 4096); }
constexpr int imin(int a, int b) { return a < b ? a : b; }
constexpr int cum(int s) { return imin(NBLK, imin(s < 1 ? 24 : 4 * s + 24, LEAD0 + 4 * (s + 1) + (s >= RS0 ? (s - RS0 + 1) * RAMP : 0))); }
constexpr int npre(int s) { return (s == H1I ? 2 : 0) + (s == H2I ? 2 : 0) + (s == H1C + HSP ? 1 : 0); }
constexpr int npost(int s) { return (s == XST ? 4 : 0); }
constexpr int issued(int s) { int n = 1 + cum(s - 1); for (int i = 0; i < s; ++i) n += npre(i) + npost(i); return n; }
constexpr int posblk(int b) { if (b < cum(-1)) return 1 + b; int s = 0; while (cum(s) <= b) ++s; return issued(s) + npre(s) + b - cum(s - 1); }
constexpr int vmring(int s) { int b = s == LASTB ? NBLK - 1 : imin(4 * s + 3 + PD, NBLK - 1); return issued(s) - posblk(b) - 1; }
constexpr int vmpre(int from, int s) { return issued(s) - (issued(from) + npre(from)); }
constexpr int gcum(int s) { constexpr int g[2 + NA] = {GSCH}; int n = 0; for (int i = 22; i < s && i < 24 + NA; ++i) n += g[i - 22]; return n; }
static_assert(cum(LASTB - 1) == NBLK && gcum(23) <= 4 && gcum(24 + NA) == 12 && LEAD0 >= 4 + PD && H2C <= 22 + NA && GE <= 191, "schedule");
constexpr int stgap(int g) { int a = 2 * gend(g) + (g < NPL ? 5 : 3); if (g < NPL && a < 290) while ((a & 7) < SGP || (a & 7) > 6) ++a; return a; }
constexpr int later(int g, int T) { int k = 0; for (int i = g + 1; i < 6; ++i) for (int j = 0; j < (i < NPL ? 2 : 4); ++j) k += stgap(i) + j < T; return k; }
struct Op { int step, val; };
constexpr Op opat(int kind, int n) {
    const int nv = kind ? 16 : 32, ns = kind ? 4 : 7, ord[2][7] = {{3, 0, 1, 5, 2, 4, 6}, {1, 0, 2, 3}}, sk[2][7] = {{0, 1, 2, 3, 4, 5, 6}, {0, 1, 1, 2}}, lag = kind ? 3 : 7;
    for (int k = 0; k < nv + lag + 2; ++k) {
        for (int j = 0; j < ns; ++j) { const int v = k - sk[kind][ord[kind][j]]; if (v >= 0 && v < nv && n-- == 0) return {ord[kind][j], v}; }
        int v = k - lag - 1; if (v >= 0 && v < nv && (v & 7) == 7 && n-- == 0) return {8, v >> 3};
        v = k - lag; if (v >= 0 && v < nv && (v & 1) && n-- == 0) return {7, v >> 1};
    }
    return {-1, 0};
}
constexpr int NOPG = 32 * 7 + 16 + 4, NOPP = 16 * 4 + 8 + 2;
static_assert(opat(0, NOPG - 1).step == 8 && opat(0, NOPG).step < 0 && opat(1, NOPP - 1).step == 8 && opat(1, NOPP).step < 0, "streams");
constexpr int part(int n, int t, int t0, int t1) { return t <= t0 ? 0 : (t >= t1 ? n : n * (t - t0) / (t1 - t0)); }
constexpr int KE = 8 * KES - 1;
constexpr int TB = 192 + 8 * NA;
constexpr int TA0 = 8 * (H2C + 1);
constexpr bool busy(int T) {
    if (T < GE || (T < 183 && (T & 7) >= 1 && (T & 7) < 7) || (T >= 192 && T < TA0 + 15) || (T >= TB && T < TB + 10)) return true;
    for (int g = 0; g < 6; ++g) if ((T >= stgap(g) - 2 && T < stgap(g) + (g < NPL ? 2 : 4))) return true;
    return false;
}
}

#ifdef __HIP_DEVICE_COMPILE__
#define NOPK __attribute__((target("no-packed-fp32-ops")))
#else
#define NOPK
#endif
__global__ __launch_bounds__(512, 2) NOPK void fused_block(const u16* __restrict__ x, const u16* __restrict__ wqkv, const u16* __restrict__ wo,
                                                      const u16* __restrict__ w1, const u16* __restrict__ b1, const u16* __restrict__ w2,
                                                      const u16* __restrict__ b2, u16* __restrict__ out, char* __restrict__ ws) {
    const int lane = threadIdx.x & 63;
    const int w = __builtin_amdgcn_readfirstlane(threadIdx.x >> 6);
    const int c = w & 3, th = c & 1, m = c >> 1;
    const int xcd = blockIdx.x & 7, jj = blockIdx.x >> 3;
    const int s = (xcd % (8 / BSL)) * BSL + (jj % BSL);
    const int t = (xcd / (8 / BSL)) * (32 / BSL) + jj / BSL;
    const int odd = s & 1;
    const int l31 = lane & 31, hf = lane >> 5;
    char* const wsF = ws + W_FL + (size_t)t * (32 * FST);
    char* const wsO = ws + W_O + (size_t)t * 475136;
    auto toff = [](int sl, int sr) INL { return (sr * 28 + sl) * 2048; };
    auto goff = [](int sl, int sr) INL { return 458752 + (sl & 3) * 4096; };
    static_assert(NPL == 6, "layout");
    char* const wsP = ws + W_P + (size_t)s * (32 * 8192);

    auto ldx = [&](u32x4* xr, u64& ep) INL {
        const char* xb = (const char*)x + (size_t)(64 * t + 8 * w) * 896;
        const u32 vx = (lane >> 3) * 896 + (lane & 7) * 16;
        sfor<0, 7>([&](auto J) INL { ld16<J.value * 128>(xr[J.value], xb, vx); });
    };
        auto touches = [&](auto Mc) INL {
            constexpr int M = Mc.value;
            const int s0 = s - (jj % BSL);
            if constexpr ((M & 1) != 0) {
                static_assert(BSL == 2 || BSL == 4, "touch map");
                const int i = l31 < 28 ? l31 : 27;
                touch((const char*)wqkv + 896 + 112 * (s0 + (c & 1)) + 896 * (c >> 1), (14 * jj + (i >> 1)) * 2688 + (i & 1) * 108 + (BSL == 4 ? hf * 224 : 0));
            }
            if ((M & 2) && c == 1) {
                const int i = lane < 56 ? lane : 55;
                const u32 v = (14 * jj + (i >> 2)) * 3584 + (i & 3) * 128;
                sfor<0, BSL>([&](auto I) INL { const int sl = s0 + I.value; touch((const char*)w1 + 448 * sl - 64 * (sl & 1), v); });
            }
            if ((M & 4) && c == 2) {
                const int i = lane < 28 ? lane : 27; int k = jj * 13 + (lane < 13 ? lane : 12); k = k < 392 ? k : 391;
                const u32 v = (14 * jj + (i >> 1)) * 2688 + (i & 1) * 108;
                sfor<0, BSL>([&](auto I) INL { const int sl = s0 + I.value; touch((const char*)wqkv + 112 * sl, v); touch((const char*)wo + sl * (56 * 896), k * 128); });
            }
            if ((M & 8) && c == 0) touch((const char*)b2, (lane < 7 ? lane : 6) * 128);
            if ((M & 8) && c == 3) {
                const u32 v = (jj * 49 + (lane < 49 ? lane : 48)) * 128;
                sfor<0, BSL>([&](auto I) INL { touch((const char*)w2 + (size_t)(s0 + I.value) * (224 * 896), v); });
            }
        };
        auto lnrow = [&](const u32x4* v, int row) INL {
            float s1 = 0.f, s2 = 0.f, s1b = 0.f, s2b = 0.f;
#pragma unroll
            for (int j = 0; j < 7; ++j) {
                s1 = dot2(v[j][0], 0x3f803f80u, s1); s2 = dot2(v[j][0], v[j][0], s2); s1b = dot2(v[j][1], 0x3f803f80u, s1b); s2b = dot2(v[j][1], v[j][1], s2b);
                s1 = dot2(v[j][2], 0x3f803f80u, s1); s2 = dot2(v[j][2], v[j][2], s2); s1b = dot2(v[j][3], 0x3f803f80u, s1b); s2b = dot2(v[j][3], v[j][3], s2b);
            }
            s1 += s1b; s2 += s2b;
            s1 += dpp<0xB1>(s1); s2 += dpp<0xB1>(s2);
            s1 += dpp<0x4E>(s1); s2 += dpp<0x4E>(s2);
            s1 += dpp<0x141>(s1); s2 += dpp<0x141>(s2);
            const float mean = s1 * (1.f / DM);
            const float var = __builtin_fmaxf(__builtin_fmaf(-mean, mean, s2 * (1.f / DM)), 0.f);
            const float rstd = __builtin_amdgcn_rsqf(var + 1e-5f);
            const f32x2 rs = {rstd, rstd}, nb = {-mean * rstd, -mean * rstd};
            const u32 xa = L_XN + row * XS + (lane & 7) * 16;
#pragma unroll
            for (int j = 0; j < 7; ++j) {
                u32x4 o;
#pragma unroll
                for (int e = 0; e < 4; ++e) {
                    const f32x2 u = __builtin_elementwise_fma((f32x2){bflo(v[j][e]), bfhi(v[j][e])}, rs, nb);
                    o[e] = cvtpk(u[0], u[1]);
                }
                lds_st<u32x4>(xa + j * 128, o);
            }
            if ((lane & 7) == 0) lds_st<float>(L_IR + row * 4, __builtin_amdgcn_sqrtf(var * rstd * rstd + 1e-5f));
        };
    if (w < 4) {
        u32x4 xr[7]; u64 epoch;
        ldx(xr, epoch);
        touches(IC<TME>{});
        sld8(epoch, ws);
        waitx<TME & 1>(xr, epoch);
        lnrow(xr, 8 * w + (lane >> 3));
        touches(IC<TMA>{});
        const u64 tag64 = epoch + 0x100000001ull;
        const u32 tag = (u32)tag64;
        bf16x8 A[28], Aext;
        const u32 xrow = L_XN + (32 * th + l31) * XS + hf * 16;
        const int bk = (lane >> 4) & 1, q = (lane & 15) >> 2, p = lane & 3;
        const u32 trrow = (8 * hf + q) * 256;
        const u32 mstd = 32 * bk + 8 * p, mperm = 32 * (p & 1) + 16 * bk + 8 * (p >> 1);
        u32 ta0, ta1, tb0, tb1;
        auto setaddr = [&](u32 cb, u32 cb1) INL {
            ta0 = trrow + (cb ^ (q << 6)); ta1 = trrow + (cb1 ^ (q << 6)); tb0 = ta0 + 3 * STB; tb1 = ta1 + 3 * STB;
            asm volatile("" : "+v"(ta0), "+v"(ta1), "+v"(tb0), "+v"(tb1));
        };
        const u32 hrow = L_H + (32 * th + l31) * HS;
        const u32 bfa = (hf == 0 && q < 2) ? L_B1 + 128 * m + mperm : (u32)L_ZERO, bfs = (hf == 0 && q < 2) ? 64u : 0u;

        constexpr int NF = 2 * PD;
        bf16x8 F[NF], Hf[18], kf0, kf1;
        s16x4 bf0, bf1;
        f32x16 P0, P1, E0, E1, G[7];
        u32 pk[8];
        const f32x16 zero16 = {0.f, 0.f, 0.f, 0.f, 0.f, 0.f, 0.f, 0.f, 0.f, 0.f, 0.f, 0.f, 0.f, 0.f, 0.f, 0.f};
        const s16x4 z4 = {0, 0, 0, 0};
        auto rdfrag = [&](auto Bc, auto Fc) INL {
            constexpr int b = Bc.value, f = Fc.value;
            if constexpr (b < NBLK) {
                if constexpr (f == 0 && b == 28) setaddr(128 * m + mperm, 128 * m + mperm + 64);
                if constexpr (f == 0 && b == 84) setaddr(56 * m + mperm, 56 * m + mperm + 128);
                if constexpr (f == 0 && b == 98) setaddr(64 * m + mperm, 64 * m + mperm + 128);
                constexpr u32 o = boff(b);
                F[(2 * b + f) % NF] = trfrag(o < 3 * STB ? (f ? ta1 : ta0) + o : (f ? tb1 : tb0) + (o - 3 * STB));
            }
        };
        u32x4 gq = {0u, tag, 0u, tag};
        u32x4 k0, k1;
        float irv, gk0, gk1, z[8], d[4];
        f32x16 Ea0, Ea1;
        u32 pw[8], paa;
        u32x4 tq[2];
        u32 pa0, pa1;
        auto slot = [&](int gi) INL { int cg = s + 1 + gi; cg = cg >= 7 ? cg - 7 : cg; cg = cg >= 7 ? cg - 7 : cg; return cg * 4 + c; };
        auto tile_st = [&](int half, int gi) INL { st16c(wsO + toff(slot(gi), s) + half * 1024, lane * 16, tq[half]); };
        auto gran_st = [&](const f32x16& a, int i, int gi) INL {
            gq[0] = cvtpk(a[4 * i], a[4 * i + 1]); gq[2] = cvtpk(a[4 * i + 2], a[4 * i + 3]);
            st16c(wsO + goff(slot(gi), s) + i * 1024, lane * 16, gq);
        };
        u32 vfl;
        auto flag_st = [&](auto Mc) INL { st8m<Mc.value>(wsF, vfl, tag64); };
        auto hook = [&](auto Tc) INL {
            constexpr int T = Tc.value;
            if constexpr (T == 52 || T == 108) { const u32 a = bfa + (((T == 52) == !odd) ? 0u : 4 * bfs); bf0 = tr4(a); bf1 = tr4(a + bfs); }
            if constexpr (T < 50 && (T & 1) == 0) A[T / 2 + 3] = lds_ld<bf16x8>(xrow + (T / 2 + 3) * 32);
            if constexpr (T == 41) irv = lds_ld<float>(L_IR + (32 * th + l31) * 4);
            if constexpr (T == 47) { const u32 hi = cvtpk(irv, 0.f); Aext = asfrag(hf ? 0u : cvtpk(irv, irv - bflo(hi)), 0u, 0u, 0u); }
            if constexpr (T == TLT && TML) touches(IC<TML>{});
            if constexpr (T == 49) { const float r2 = __builtin_amdgcn_rcpf(irv); gk0 = -2.f * 0.7978845608028654f * 1.4426950408889634f * r2; gk1 = r2 * r2; pin(gk0); pin(gk1); }
            if constexpr (T == 53) { gk1 *= gk0 * 0.044715f; pin(gk1); }
            auto phis = [&](auto N) INL {
                constexpr Op o = opat(1, N.value); constexpr int v = o.val;
                if constexpr (o.step == 0) { z[v & 3] = E0[v] * 1.4426950408889634f; pin(z[v & 3]); }
                if constexpr (o.step == 1) { z[v & 3] = __builtin_amdgcn_fmed3f(__builtin_amdgcn_exp2f(z[v & 3]), 0.f, 1.f); pin(z[v & 3]); }
                if constexpr (o.step == 2) { d[v & 3] = __builtin_amdgcn_fmed3f(E0[v], 1e-6f, 3.0e38f); pin(d[v & 3]); }
                if constexpr (o.step == 3) { d[v & 3] += z[v & 3]; pin(d[v & 3]); }
                if constexpr (o.step == 7) { pk[v] = cvtpk(d[(2 * v) & 3], d[(2 * v + 1) & 3]); pin(pk[v]); }
                if constexpr (o.step == 8 && T < 100) lds_st<u32x4>(pa0 + v * 1024, (u32x4){pk[4 * v], pk[4 * v + 1], pk[4 * v + 2], pk[4 * v + 3]});
            };
            if constexpr (T >= 57 && T < KE) {
                sfor<part(NOPP, T, 57, KE), part(NOPP, T + 1, 57, KE)>(phis);
                sfor<part(8, T, 58, KE - 3), part(8, T + 1, 58, KE - 3)>([&](auto I) INL {
                    constexpr int i = I.value;
                    pw[i & 3] = cvtpk(E1[2 * i], E1[2 * i + 1]);
                    if constexpr ((i & 3) == 3) lds_st<u32x4>(pa0 + 2048 + (i >> 2) * 1024, (u32x4){pw[0], pw[1], pw[2], pw[3]}); else pin(pw[i & 3]);
                });
            }
            if constexpr (T == 112) { Ea0 = E0; Ea1 = E1; }
            if constexpr (T >= 113 && T < GE) sfor<part(NOPG, T, 113, GE), part(NOPG, T + 1, 113, GE)>([&](auto N) INL {
                constexpr Op o = opat(0, N.value); constexpr int v = o.val;
                if constexpr (o.step < 7) {
                    const float a = v < 16 ? Ea0[v & 15] : Ea1[v & 15];
                    float& y = z[v & 7];
                    if constexpr (o.step == 0) y = a * a;
                    if constexpr (o.step == 1) y = __builtin_fmaf(y, gk1, gk0);
                    if constexpr (o.step == 2) y *= a;
                    if constexpr (o.step == 3) y = __builtin_amdgcn_exp2f(y);
                    if constexpr (o.step == 4) y = __builtin_fmaf(y, irv, irv);
                    if constexpr (o.step == 5) y = __builtin_amdgcn_rcpf(y);
                    if constexpr (o.step == 6) { d[v & 3] = y * a; pin(d[v & 3]); } else pin(y);
                }
                if constexpr (o.step == 7) { pw[v & 7] = cvtpk(d[(2 * v) & 3], d[(2 * v + 1) & 3]); pin(pw[v & 7]); }
                if constexpr (o.step == 8) { constexpr int k = 4 * (v & 1); lds_st<u32x4>(paa + 16 * (v & 1) + 64 * (v >> 1), (u32x4){pw[k], pw[k + 1], pw[k + 2], pw[k + 3]}); }
            });
            if constexpr (T >= 168 && T < 184 && (T & 7) >= 1 && (T & 7) < 7) {
                constexpr int j = (T - 1) & 7, e1 = T >= 176 ? 8 : 0, lo = e1 + (4 * j) / 3, hi = e1 + (4 * j + 4) / 3;
                sfor<lo, hi>([&](auto I) INL {
                    constexpr int i = I.value; const f32x16& e = i < 8 ? E0 : E1;
                    pk[i & 3] = cvtpk(e[2 * (i & 7)], e[2 * (i & 7) + 1]);
                    if constexpr ((i & 3) == 3) lds_st<u32x4>((i < 8 ? pa0 : pa1) + 16 * ((i >> 2) & 1), (u32x4){pk[0], pk[1], pk[2], pk[3]});
                    else pin(pk[i & 3]);
                });
            }
            if constexpr (T == 51) { pa0 = L_KVS + c * 4096 + lane * 16; asm volatile("" : "+v"(pa0)); }
            if constexpr (T == 101) { paa = hrow + 128 * m + 32 * hf; pin(paa); }
            if constexpr (T == 161) {
                const int bi = 2 * m - odd;
                pa0 = bi < 0 ? (u32)L_DUMMY : hrow + 256 + 64 * bi + 32 * hf; pa1 = bi > 1 ? (u32)L_DUMMY : hrow + 320 + 64 * bi + 32 * hf;
                asm volatile("" : "+v"(pa0), "+v"(pa1));
            }
            if constexpr (T == 8 * (KES + KSP) + 8) { k0 = lds_ld<u32x4>(L_KVG + c * 2048 + lane * 16); k1 = lds_ld<u32x4>(L_KVG + c * 2048 + 1024 + lane * 16); }
            if constexpr (T == KST || T == KST + 1) st16c(wsP + (8 * c + (T == KST ? 0 : 4)) * 8192 + t * 256, (lane >> 4) * 8192 + (lane & 15) * 16, T == KST ? k0 : k1);
            if constexpr (T >= 192 && T < 200) Hf[T - 192] = lds_ld<bf16x8>(hrow + hf * 16 + (T - 192) * 32);
            if constexpr (T >= TB && T < TB + 6) Hf[T - TB + 8] = lds_ld<bf16x8>(hrow + hf * 16 + (T - TB + 8) * 32);
            if constexpr (T == TB + 8 || T == TB + 9) { constexpr int i = 14 + 2 * (T - TB - 8); Hf[i] = lds_ld<bf16x8>(hrow + hf * 16 + i * 32); Hf[i + 1] = lds_ld<bf16x8>(hrow + hf * 16 + (i + 1) * 32); }
            if constexpr (T >= 198 && T < TA0 + 2) sfor<part(NOPP, T, 198, TA0 + 2), part(NOPP, T + 1, 198, TA0 + 2)>(phis);
            if constexpr (T == TA0) {
                const int e = 16 * ((l31 >> 2) & 1) + 4 * (l31 >> 3) + (l31 & 3);
                const u32 ka = L_KVB + m * 2560 + e * 80 + hf * 32;
                kf0 = lds_ld<bf16x8>(ka); kf1 = lds_ld<bf16x8>(ka + 16);
            }
            if constexpr (T == TA0 + 2) E0 = MFMA32(kf0, asfrag(pk[0], pk[1], pk[2], pk[3]), zero16);
            if constexpr (T == TA0 + 3) E0 = MFMA32(kf1, asfrag(pk[4], pk[5], pk[6], pk[7]), E0);
            if constexpr (T == TA0 + 4) { pa0 = hrow + 448 + m * 56 + hf * 32; pa1 = hf ? (u32)L_DUMMY : pa0 + 24; pin(pa0); pin(pa1); }
            if constexpr (T == TA0 + 5) {
                auto dd = __builtin_amdgcn_permlane32_swap(__float_as_uint(E0[12]), __float_as_uint(E0[12]), false, false);
                irv = __uint_as_float(dd[1]) + 1e-6f; pin(irv);
            }
            if constexpr (T == TA0 + 6) { irv = __builtin_amdgcn_rcpf(irv); pin(irv); }
            if constexpr (T >= TA0 + 7 && T < TA0 + 15) {
                constexpr int i = (T - TA0 - 7) >> 1;
                if constexpr ((T - TA0) & 1) sfor<0, 4>([&](auto J) INL { d[J.value] = E0[4 * i + J.value] * irv; pin(d[J.value]); });
                else lds_st<u32x2>(i == 3 ? pa1 : pa0 + i * 8, (u32x2){cvtpk(d[0], d[1]), cvtpk(d[2], d[3])});
            }
            if constexpr (T == 185) { vfl = slot(lane & 7) * FST + s * 8; pin(vfl); }
            sfor<0, 6>([&](auto Gc) INL {
                constexpr int g = Gc.value, j = T - stgap(g);
                if constexpr (g < NPL && (j == -2 || j == -1)) {
                    constexpr int o = 8 * (j + 2); const f32x16& a = G[g];
                    tq[j + 2] = (u32x4){cvtpk(a[o], a[o + 1]), cvtpk(a[o + 2], a[o + 3]), cvtpk(a[o + 4], a[o + 5]), cvtpk(a[o + 6], a[o + 7])}; pin(tq[j + 2]);
                }
                if constexpr (g < NPL && (j == 0 || j == 1)) tile_st(j, g);
                if constexpr (g >= NPL && j >= 0 && j < 4) gran_st(G[g], j, g);
            });
        };
        auto mm = [&](auto Bc, auto Fc) INL {
            constexpr int b = Bc.value, f = Fc.value; constexpr Blk k = blk(b);
            const bf16x8 W = F[(2 * b + f) % NF];
            f32x16& P = f ? P1 : P0;
            if constexpr (k.kind == K_KV) P = MFMA32(A[k.k], W, k.k ? P : zero16);
            else if constexpr (k.kind == K_WA || k.kind == K_WB) {
                if constexpr (k.k == 0 && f == 0) { E0 = P0; E1 = P1; }
                if constexpr (k.k == 0) { P = MFMA32(__builtin_shufflevector(f ? bf1 : bf0, z4, 0, 1, 2, 3, 4, 5, 6, 7), Aext, zero16); }
                P = MFMA32(W, A[k.k], P);
            } else if constexpr (k.kind == K_Q) {
                if constexpr (k.k == 0 && f == 0) { E0 = P0; E1 = P1; }
                P0 = MFMA32(W, A[2 * k.k + f], (k.k || f) ? P0 : zero16);
            } else {
                if constexpr (b == 98 && f == 0) E0 = P0;
                G[k.grp] = MFMA32(W, Hf[2 * k.k + f], (k.k || f) ? G[k.grp] : zero16);
            }
        };
        setaddr(56 * m + mstd, 56 * m + mstd + 128);
        sfor<0, NBLK>([&](auto Bc) INL {
            constexpr int b = Bc.value;
            if constexpr (b % 4 == 0 && b / 4 <= LASTB) {
                constexpr int sg = b / 4;
                constexpr int lg = sg == 0 ? 0 : ((sg == H2C + 3 || sg == KES || sg == 22 || sg == 23 || sg == 24) ? 2 : 15);
                SBAR();
                if constexpr (lg < 15) asm volatile("s_waitcnt lgkmcnt(%0)\n\ts_barrier" :: "n"(lg) : "memory");
                else asm volatile("s_barrier" ::: "memory");
            SBAR();
                if constexpr (b == 0) {
                    sfor<0, PD>([&](auto I) INL { A[I.value] = lds_ld<bf16x8>(xrow + I.value * 32); rdfrag(I, IC<0>{}); rdfrag(I, IC<1>{}); SBAR(); });
                    sfor<PD, 3>([&](auto I) INL { A[I.value] = lds_ld<bf16x8>(xrow + I.value * 32); });
                    SBAR();
                }
            }
            constexpr bool dep = blk(b).kind >= K_Q;
            mm(Bc, IC<0>{}); SBAR(); rdfrag(IC<b + PD>{}, IC<0>{}); SBAR(); hook(IC<2 * b>{}); SBAR();
            if constexpr (dep && !busy(2 * b)) sfor<0, PADN>([&](auto) INL { asm volatile("s_nop 0"); });
            SBAR();
            mm(Bc, IC<1>{}); SBAR(); rdfrag(IC<b + PD>{}, IC<1>{}); SBAR(); hook(IC<2 * b + 1>{}); SBAR();
            if constexpr (dep && !busy(2 * b + 1)) sfor<0, PADN>([&](auto) INL { asm volatile("s_nop 0"); });
            SBAR();
        });
        constexpr int NL = later(NPL - 1, 999);
        if (s == 7) {
            gran_st(G[6], 0, 6); gran_st(G[6], 1, 6); gran_st(G[6], 2, 6); gran_st(G[6], 3, 6);
            waitvm<NL + 4>();
        } else {
#pragma unroll
            for (int i = 0; i < 4; ++i) lds_st<f32x4>(L_KVS + c * 4096 + i * 1024 + lane * 16, (f32x4){G[6][4 * i], G[6][4 * i + 1], G[6][4 * i + 2], G[6][4 * i + 3]});
            waitvm<NL>();
        }
        flag_st(IC<(1 << NPL) - 1>{});
        SBAR();
        asm volatile("s_waitcnt lgkmcnt(0)\n\ts_barrier" ::: "memory");
            SBAR();
    } else {
        const int h = c;
        u32x4 xr[7]; u64 epoch;
        ldx(xr, epoch);
        const int r4 = lane >> 4, ch = (lane & 15) ^ (r4 << 2), seg = ch >> 3, c8 = ch & 7, row = 4 * h + r4, c7 = c8 > 6 ? 6 : c8;
        const u32 vKV = row * 2688 + seg * 896 + c7 * 16, vW1 = row * 3584 + ch * 16, vQ = (row + 16 * seg) * 2688 + c7 * 16;
        const u32 vW2 = (row + 16 * seg) * 896 + c8 * 16, vWo = (row + 16 * seg > 23 ? 23 : row + 16 * seg) * 896 + c8 * 16;
        const u16 *const rQ = wqkv, *const r1 = w1, *const r2 = w2, *const rO = wo;
        const u32 gQ = 112 * s, wb = 448 * s - 64 * odd, gWa = wb + 256 * odd, gWb = wb + 256 * (1 - odd);
        const u32 g2a = (224 * s + 96 * odd) * 896, g2b = (224 * s + 128 * (1 - odd)) * 896, gWo = s * (56 * 896);
        const u32 ldsw = h * 1024;
        auto issue = [&](auto Bc) INL {
            constexpr int b = Bc.value;
            if constexpr (b < NBLK) {
                constexpr Blk k = blk(b); constexpr int IMM = boff(b);
                if constexpr (k.kind == K_KV) glds16<IMM, 896 + k.k * 16 * 2688>(rQ, gQ, vKV, ldsw);
                else if constexpr (k.kind == K_WA) glds16<IMM, k.k * 16 * 3584>(r1, gWa, vW1, ldsw);
                else if constexpr (k.kind == K_WB) glds16<IMM, k.k * 16 * 3584>(r1, gWb, vW1, ldsw);
                else if constexpr (k.kind == K_Q) glds16<IMM, k.k * 32 * 2688>(rQ, gQ, vQ, ldsw);
                else {
                    int cg = s + 1 + k.grp; cg = cg >= 7 ? cg - 7 : cg; cg = cg >= 7 ? cg - 7 : cg;
                    if constexpr (k.k < 4) glds16<IMM, k.k * 32 * 896>(r2, g2a + cg * 128, vW2, ldsw);
                    else if constexpr (k.k < 7) glds16<IMM, (k.k - 4) * 32 * 896>(r2, g2b + cg * 128, vW2, ldsw);
                    else glds16<IMM, (k.k - 7) * 32 * 896>(rO, gWo + cg * 128, k.k == 7 ? vW2 : vWo, ldsw);
                }
            }
        };
        if (h == 0 && lane < 4) lds_st<u32>(L_ZERO + lane * 4, 0u);
        glds16<L_B1, 0>(b1, wb, l31 * 16, 0u);
        sfor<0, 2>(issue);
        sld8(epoch, ws);
        sfor<2, LEADA>(issue);
        waitx<1 + LEADA>(xr, epoch);
        lnrow(xr, 8 * w + (lane >> 3));
        sfor<LEADA, cum(-1)>(issue);
        const u64 tag64 = epoch + 0x100000001ull;
        const u32 tag = (u32)tag64;
        char* const wsR = ws + W_R + (size_t)s * 8192;
        float ir, gk0, gk1;
        const char* const p1 = wsP + (size_t)t * 8192 + h * 64;
        const u32 v1 = (lane >> 2) * 256 + (lane & 3) * 16;
        auto gelun = [&](u32x4& r, auto Oc, auto Nc) INL {
            constexpr int o = Oc.value, n = 2 * Nc.value;
            float a[n], z[n];
            sfor<0, n>([&](auto I) INL { a[I.value] = (I.value & 1) ? bfhi(r[o + I.value / 2]) : bflo(r[o + I.value / 2]); }); SBAR();
            sfor<0, n>([&](auto I) INL { z[I.value] = a[I.value] * a[I.value]; }); SBAR();
            sfor<0, n>([&](auto I) INL { z[I.value] = __builtin_fmaf(z[I.value], gk1, gk0); }); SBAR();
            sfor<0, n>([&](auto I) INL { z[I.value] *= a[I.value]; }); SBAR();
            sfor<0, n>([&](auto I) INL { z[I.value] = __builtin_amdgcn_exp2f(z[I.value]); }); SBAR();
            sfor<0, n>([&](auto I) INL { z[I.value] = __builtin_fmaf(z[I.value], ir, ir); }); SBAR();
            sfor<0, n>([&](auto I) INL { z[I.value] = __builtin_amdgcn_rcpf(z[I.value]); }); SBAR();
            sfor<0, n>([&](auto I) INL { z[I.value] *= a[I.value]; }); SBAR();
            sfor<0, n / 2>([&](auto I) INL { r[o + I.value] = cvtpk(z[2 * I.value], z[2 * I.value + 1]); }); SBAR();
        };
        const u32 hrb = L_H + lane * HS + 256 + h * 16;
        auto uaddr = [&](int u) INL { return u == 0 ? hrb + 64 * odd : (u == 1 ? hrb + 64 - 64 * odd : hrb + 128); };
        u32x4 ha, hb, xv0, xv1, bv0, bv1, raw[6];
        const f32x4 z4 = {0.f, 0.f, 0.f, 0.f};
        f32x4 kv0, kv1;
        float ra, rb;
        auto kvout = [&]() INL {
            lds_st<u32x4>(L_KVG + h * 2048 + lane * 16, (u32x4){cvtpk(kv0[0], kv0[1]), tag, cvtpk(kv0[2], kv0[3]), tag});
            lds_st<u32x4>(L_KVG + h * 2048 + 1024 + lane * 16, (u32x4){cvtpk(kv1[0], kv1[1]), tag, cvtpk(kv1[2], kv1[3]), tag});
        };
        const int G0 = s < 7 ? s : 6;
        const char* const fl = wsF + (G0 * 4 + h) * FST;
        sfor<0, LASTB + 1>([&](auto Sc) INL {
            constexpr int sg = Sc.value;
            constexpr int vr = vmring(sg), vh = sg == H1C ? vmpre(H1I, sg) : (sg == H2C ? vmpre(H2I, sg) : 63), vm = vh < vr ? vh : vr;
            constexpr int lg = (sg == 0 || sg == KES + 1 + KSP || sg == 24 + NA || sg == H2C + 1) ? 0 : 15;
            SBAR();
            if constexpr (sg == H1C || sg == H2C) asm volatile("s_waitcnt vmcnt(%2) lgkmcnt(%3)\n\ts_barrier" : "+v"(ha), "+v"(hb) : "n"(vm), "n"(lg) : "memory");
            else asm volatile("s_waitcnt vmcnt(%0) lgkmcnt(%1)\n\ts_barrier" :: "n"(vm), "n"(lg) : "memory");
            SBAR();
            if constexpr (sg == KES) {
                const u32 a = L_KVS + m * 8192 + (lane >> 5) * 1024 + (((lane >> 4) & 1) * 32 + (lane & 15)) * 16;
                raw[0] = lds_ld<u32x4>(a + th * 256); raw[2] = lds_ld<u32x4>(a + 2048); raw[4] = lds_ld<u32x4>(a + 2048 + 256);
                raw[1] = lds_ld<u32x4>(a + th * 256 + 4096); raw[3] = lds_ld<u32x4>(a + 6144); raw[5] = lds_ld<u32x4>(a + 6144 + 256);
            }
            constexpr int g0 = gcum(sg), g1 = gcum(sg + 1);
            sfor<0, 3>([&](auto U) INL {
                constexpr int u = U.value, av = u == 0 ? 22 : 23;
                constexpr int due = [] { int k = 22; while (gcum(k + 1) <= 4 * u) ++k; return k; }();
                if constexpr (sg == (due - 1 > av ? due - 1 : av)) raw[u] = lds_ld<u32x4>(uaddr(u));
            });
            auto gwork = [&](auto U) INL {
                constexpr int u = U.value, lo = g0 > 4 * u ? g0 : 4 * u, hi = g1 < 4 * u + 4 ? g1 : 4 * u + 4;
                if constexpr (lo < hi) {
                    gelun(raw[u], IC<lo - 4 * u>{}, IC<hi - lo>{});
                    if constexpr (hi == 4 * u + 4) lds_st<u32x4>(uaddr(u), raw[u]);
                    else asm volatile("" : "+v"(raw[u]));
                }
            };
            if constexpr (sg == H1I) { ld16c<0>(ha, p1, v1); ld16c<0>(hb, p1 + 4096, v1); }
            if constexpr (sg == H2I) { const char* p = wsR + h * 2048; ld16c<0>(ha, p, lane * 16); ld16c<1024>(hb, p, lane * 16); }
            if constexpr (KSP && sg == KES + 1) kvout();
            if constexpr (sg == H1C || sg == H2C) {
                const char* p = sg == H1C ? p1 : wsR + h * 2048;
                const u32 vo = sg == H1C ? v1 : lane * 16;
                u32 spin = 0;
#pragma unroll 1
                for (; spin < SPIN_CAP; ++spin) {
                    if (wall(ha[1] == tag && ha[3] == tag && hb[1] == tag && hb[3] == tag)) break;
                    ld16c<0>(ha, p, vo); ld16c<0>(hb, p + (sg == H1C ? 4096 : 1024), vo); waitvm2<0>(ha, hb);
                }
            }
            if constexpr (sg == H1C) {
                auto r0 = __builtin_amdgcn_permlane32_swap(__float_as_uint(bflo(ha[0]) + bflo(hb[0])), __float_as_uint(bflo(ha[2]) + bflo(hb[2])), false, false);
                auto r1 = __builtin_amdgcn_permlane32_swap(__float_as_uint(bfhi(ha[0]) + bfhi(hb[0])), __float_as_uint(bfhi(ha[2]) + bfhi(hb[2])), false, false);
                ra = __uint_as_float(r0[0]) + __uint_as_float(r0[1]); rb = __uint_as_float(r1[0]) + __uint_as_float(r1[1]);
                if constexpr (HSP) { pin(ra); pin(rb); }
            }
            if constexpr (sg == H1C + HSP) {
                float a = ra, b = rb;
                auto q0 = __builtin_amdgcn_permlane16_swap(__float_as_uint(a), __float_as_uint(a), false, false);
                auto q1 = __builtin_amdgcn_permlane16_swap(__float_as_uint(b), __float_as_uint(b), false, false);
                a = __uint_as_float(q0[0]) + __uint_as_float(q0[1]); b = __uint_as_float(q1[0]) + __uint_as_float(q1[1]);
                a += dpp<0x128>(a); b += dpp<0x128>(b);
                a += dpp<0x124>(a); b += dpp<0x124>(b);
                const u32x2 gr = {cvtpk(a, b), tag};
                asm volatile("s_mov_b64 exec, %3\n\tglobal_store_dwordx2 %0, %1, %2 sc1\n\ts_mov_b64 exec, -1"
                             :: "v"((u32)((lane & 3) * 16 + hf * 8)), "v"(gr), "s"(wsR + t * 256 + h * 64), "s"(0x0000000f0000000full) : "memory");
            }
            if constexpr (sg == H2C) {
                const u32 a = L_KVB + m * 2560 + (lane & 15) * 80 + th * 32 + r4 * 8;
                const bool z = th && r4 == 3;
                lds_st<u32x2>(a, (u32x2){z ? 0u : ha[0], z ? 0u : ha[2]});
                lds_st<u32x2>(a + 1280, (u32x2){z ? 0u : hb[0], z ? 0u : hb[2]});
            }
            SBAR(); sfor<cum(sg - 1), cum(sg)>(issue);
            SBAR(); sfor<0, 3>(gwork); SBAR();
            if constexpr (sg == KES + 2 + KSP) { if (h == 1) lds_st<u32x4>(L_H + lane * HS + 560, (u32x4){0u, 0u, 0u, 0u}); }
            if constexpr (sg == KES) {
#pragma unroll
                for (int e = 0; e < 4; ++e) { raw[4][e] = (lane & 15) == 12 ? 0x3f803f80u : raw[4][e]; raw[5][e] = (lane & 15) == 12 ? 0x3f803f80u : raw[5][e]; }
                auto fr = [&](int i) INL { return __builtin_bit_cast(bf16x8, raw[i]); };
                kv0 = MFMA16(fr(0), fr(2), z4); kv1 = MFMA16(fr(0), fr(4), z4);
                kv0 = MFMA16(fr(1), fr(3), kv0); kv1 = MFMA16(fr(1), fr(5), kv1);
                if constexpr (!KSP) kvout();
            }
            if constexpr (sg == 8) ir = lds_ld<float>(L_IR + lane * 4);
            if constexpr (sg == 13) {
                const float r2 = __builtin_amdgcn_rcpf(ir);
                gk0 = -2.f * 0.7978845608028654f * 1.4426950408889634f * r2; gk1 = gk0 * 0.044715f * r2 * r2;
                asm volatile("" : "+v"(gk0), "+v"(gk1));
            }
            if constexpr (sg == XST) {
                const char* px = (const char*)(x + (size_t)(64 * t + 32 * th) * DM + 64 * G0 + 32 * m);
                const char* pb = (const char*)(b2 + 64 * G0 + 32 * m);
                ld16<0>(xv0, px, l31 * 896 + hf * 32); ld16<16>(xv1, px, l31 * 896 + hf * 32); ld16<0>(bv0, pb, hf * 32); ld16<16>(bv1, pb, hf * 32);
            }
        });
        asm volatile("s_waitcnt vmcnt(0)" : "+v"(xv0), "+v"(xv1), "+v"(bv0), "+v"(bv1) :: "memory");
        if (s < 7) {
            f32x2 f[8];
            auto up = [&](u32 v) INL { return (f32x2){bflo(v), bfhi(v)}; };
            auto pinf = [&]() INL { asm volatile("" : "+v"(f[0]), "+v"(f[1]), "+v"(f[2]), "+v"(f[3]), "+v"(f[4]), "+v"(f[5]), "+v"(f[6]), "+v"(f[7])); };
            auto sptr = [&](int k) INL { int sr = s - 1 - k; sr = sr < 0 ? sr + 7 : sr; return wsO + (k < NPL ? toff(s * 4 + h, sr) : goff(s * 4 + h, sr)); };
            const int g7 = s ? s - 1 : 6;
            const char* p7 = wsO + (g7 < NPL ? toff(s * 4 + h, 7) : goff(s * 4 + h, 7));
            int pos = s - 1 - (lane & 7); pos = pos < 0 ? pos + 7 : pos; pos = (lane & 7) == 7 ? (g7 < NPL ? NPL - 1 : 6) : pos;
            constexpr int NG = 6 - NPL;
            u32x4 pv[2 * NPL], gb[4 * NG + 1], q7[4];
            auto ld4 = [&](u32x4* g, const char* p) INL { ld16c<0>(g[0], p, lane * 16); ld16c<1024>(g[1], p, lane * 16); ld16c<2048>(g[2], p, lane * 16); ld16c<3072>(g[3], p, lane * 16); };
            auto fin = [&](u32x4* g, const char* p, auto L) INL {
                u32 spin = 0;
#pragma unroll 1
                for (; spin < SPIN_CAP; ++spin) {
                    u32 d = 0;
#pragma unroll
                    for (int i = 0; i < 4; ++i) d |= (g[i][1] ^ tag) | (g[i][3] ^ tag);
                    if (wall(d == 0)) break;
                    ld4(g, p); waitvm4<0>(g);
                }
#pragma unroll
                for (int i = 0; i < 4; ++i) { f[2 * i] += up(g[i][0]); f[2 * i + 1] += up(g[i][2]); }
                pinf();
            };
            auto addp = [&](const u32x4& a, const u32x4& b) INL {
#pragma unroll
                for (int e = 0; e < 4; ++e) { f[e] += up(a[e]); f[4 + e] += up(b[e]); }
                pinf();
            };
            const char* pp[NPL]; const char* p7b = g7 < NPL ? p7 - 2048 : p7; u64 m7 = g7 < NPL ? 1ull : ~0ull;
            sfor<0, NPL>([&](auto K) INL { pp[K.value] = sptr(K.value); pins(pp[K.value]); });
            pins(p7); pins(p7b); pins(m7);
            auto ldp = [&](auto K) INL { const char* p = pp[K.value]; ld16c<0>(pv[2 * K.value], p, lane * 16); ld16c<1024>(pv[2 * K.value + 1], p, lane * 16); };
#pragma unroll
            for (int e = 0; e < 4; ++e) { f[e] = up(xv0[e]) + up(bv0[e]); f[4 + e] = up(xv1[e]) + up(bv1[e]); }
            pinf();
            constexpr int N1 = 0;
            SBAR();
            asm volatile("s_barrier" ::: "memory");
            SBAR();
            u64 fv;
            if (PS0) __builtin_amdgcn_s_sleep(PS0);
            ld8c(fv, fl, (lane & 7) * 8);
#pragma unroll
            for (int i = 0; i < 4; ++i) {
                const f32x4 v = lds_ld<f32x4>(L_KVS + h * 4096 + i * 1024 + lane * 16);
                f[2 * i] += (f32x2){v[0], v[1]}; f[2 * i + 1] += (f32x2){v[2], v[3]};
            }
            pinf();
            wait8(fv);
            u32 spin = 0;
#pragma unroll 1
            for (; spin < SPIN_CAP; ++spin) {
                if (wall(fv == tag64 || pos >= NPL)) break;
                ld8c(fv, fl, (lane & 7) * 8); wait8(fv);
            }
            sfor<N1, NPL>(ldp);
            sfor<0, NG>([&](auto K) INL { ld4(gb + 4 * K.value, sptr(NPL + K.value)); });
            ld16c<0>(q7[0], p7, lane * 16); ld16c<1024>(q7[1], p7, lane * 16);
            ld2m(q7[2], q7[3], p7b, lane * 16, m7);
            sfor<N1, NPL>([&](auto K) INL { waitvm2<2 * (NPL - 1 - K.value) + 4 * NG + 4>(pv[2 * K.value], pv[2 * K.value + 1]); addp(pv[2 * K.value], pv[2 * K.value + 1]); });
            sfor<0, NG>([&](auto K) INL { waitvm4<0>(gb + 4 * K.value); });
            waitvm4<0>(q7);
            sfor<0, NG>([&](auto K) INL { fin(gb + 4 * K.value, sptr(NPL + K.value), IC<58>{}); });
            if (g7 < NPL) addp(q7[0], q7[1]); else fin(q7, p7, IC<59>{});
            char* po = (char*)(out + (size_t)(64 * t + 32 * th) * DM + 64 * s + 32 * m);
            sto<0>(po, l31 * 896 + hf * 32, (u32x4){cvtpk(f[0][0], f[0][1]), cvtpk(f[1][0], f[1][1]), cvtpk(f[2][0], f[2][1]), cvtpk(f[3][0], f[3][1])});
            sto<16>(po, l31 * 896 + hf * 32, (u32x4){cvtpk(f[4][0], f[4][1]), cvtpk(f[5][0], f[5][1]), cvtpk(f[6][0], f[6][1]), cvtpk(f[7][0], f[7][1])});
            if (blockIdx.x == 0 && w == 7 && lane == 0) st8c(ws, 0u, (u32x2){tag, (u32)(tag64 >> 32)});
        } else {
            SBAR();
            asm volatile("s_barrier" ::: "memory");
            SBAR();
        }
    }
}

extern "C" void kernel_launch(void* const* d_in, const int* in_sizes, int n_in, void* d_out, int out_size, void* d_ws, size_t ws_size,
                              hipStream_t stream) {
    (void)in_sizes; (void)n_in; (void)out_size; (void)ws_size;
    (void)hipFuncSetAttribute((const void*)fused_block, hipFuncAttributeMaxDynamicSharedMemorySize, LDS_BYTES);
    hipLaunchKernelGGL(fused_block, dim3(256), dim3(512), LDS_BYTES, stream,
                       (const u16*)d_in[0], (const u16*)d_in[1], (const u16*)d_in[2], (const u16*)d_in[4], (const u16*)d_in[5],
                       (const u16*)d_in[6], (const u16*)d_in[7], (u16*)d_out, (char*)d_ws);
}
```
